```python
import torch

def _softmax(x, axis=-1):
    return torch.softmax(x, dim=axis)
def _gelu(x):
    return torch.nn.functional.gelu(x, approximate="tanh")
def _silu(x):
    return torch.nn.functional.silu(x)
def _sigmoid(x):
    return torch.sigmoid(x)
def _layernorm(x, axis=-1, eps=1e-5):
    m = x.mean(dim=axis, keepdim=True)
    v = x.var(dim=axis, keepdim=True, unbiased=False)
    return (x - m) / torch.sqrt(v + eps)
def _scatter_add(tgt, idx, src, axis=0):
    return tgt.clone().index_add_(axis, idx, src)
def _segment_sum(src, seg, n):
    out = torch.zeros((n,) + src.shape[1:], dtype=src.dtype)
    return out.index_add_(0, seg, src)
def _pad(x, pw):
    flat = tuple(v for pair in reversed(pw) for v in pair)
    return torch.nn.functional.pad(x, flat)

kernel_name = 'standalone__swiglu_ffn__prompt'

def setup_inputs():

    _w = (lambda a: (lambda t: t.to(torch.bfloat16) if t.is_floating_point() else t)(torch.as_tensor(a)))
    B, S, D, F = 1, 2048, 512, 1376
    return {
        "x": _w((lambda *s: torch.randn(*s, dtype=torch.bfloat16) * 0.05)(B, S, D)),
        "w_gate": _w((lambda *s: torch.randn(*s, dtype=torch.bfloat16) * 0.05)(D, F)),
        "w_up": _w((lambda *s: torch.randn(*s, dtype=torch.bfloat16) * 0.05)(D, F)),
        "w_down": _w((lambda *s: torch.randn(*s, dtype=torch.bfloat16) * 0.05)(F, D)),
    }

def reference(x, w_gate, w_up, w_down):
    g = _silu(x @ w_gate)
    u = x @ w_up
    return (g * u) @ w_down
```

```cpp
#include <hip/hip_runtime.h>
#include <hip/hip_bf16.h>

typedef short bf16x8 __attribute__((ext_vector_type(8)));
typedef short s16x4 __attribute__((ext_vector_type(4)));
typedef float f32x4 __attribute__((ext_vector_type(4)));
typedef float f32x16 __attribute__((ext_vector_type(16)));
typedef float f32x2 __attribute__((ext_vector_type(2)));
typedef __bf16 bf16x2_t __attribute__((ext_vector_type(2)));
typedef unsigned v4u __attribute__((ext_vector_type(4)));
typedef unsigned v2u __attribute__((ext_vector_type(2)));
#define LAS __attribute__((address_space(3)))
#define GAS __attribute__((address_space(1)))
#define DI __device__ __forceinline__

constexpr int M_ = 2048, D_ = 512, F_ = 1376;
constexpr int WROW = F_ * 2;
constexpr int SLOT = 40960, WOFF = 16384;
constexpr int NKS = F_ / 16;
constexpr int WS_EPOCH = 0;
constexpr int WS_FLAGS = 4096;
constexpr int WS_H = 65536;
constexpr int WS_BYTES = WS_H + 64 * NKS * 1024;
constexpr int AUX_SC1 = 16;

DI unsigned rfl(unsigned x) { return __builtin_amdgcn_readfirstlane(x); }
DI void dma16(unsigned m0v, unsigned voff, __amdgpu_buffer_rsrc_t rs, unsigned soff) {
  asm volatile("s_mov_b32 m0, %0\n\ts_nop 0\n\tbuffer_load_dwordx4 %1, %2, %3 offen lds"
               :: "s"(m0v), "v"(voff), "s"(rs), "s"(soff) : "memory");
}
DI unsigned cvtpk(float lo, float hi) {
  f32x2 v = {lo, hi};
  bf16x2_t b = __builtin_convertvector(v, bf16x2_t);
  return __builtin_bit_cast(unsigned, b);
}
DI bf16x8 trfrag(unsigned addr0, unsigned addr1) {
  s16x4 a = __builtin_amdgcn_ds_read_tr16_b64_v4i16((LAS s16x4*)addr0);
  s16x4 b = __builtin_amdgcn_ds_read_tr16_b64_v4i16((LAS s16x4*)addr1);
  return __builtin_shufflevector(a, b, 0, 1, 2, 3, 4, 5, 6, 7);
}
DI float silu_mul(float g, float u) {
  float e = __builtin_amdgcn_exp2f(g * -1.44269504088896340736f);
  float s = g * __builtin_amdgcn_rcpf(1.0f + e);
  return s * u;
}
#define WAIT_BAR(N) asm volatile("s_waitcnt vmcnt(" #N ") lgkmcnt(0)\n\ts_barrier" ::: "memory")
#define WAIT_V(N) asm volatile("s_waitcnt vmcnt(" #N ")" ::: "memory")

#define STAMP(i)

__global__ __launch_bounds__(512, 2) void ffn_fused(const unsigned short* __restrict__ x, const unsigned short* __restrict__ wg,
                                                    const unsigned short* __restrict__ wu, const unsigned short* __restrict__ wd,
                                                    unsigned short* __restrict__ out, unsigned char* __restrict__ ws
                                                    ) {
  extern __shared__ __attribute__((aligned(16))) char lds_[];
  const unsigned ldsb = (unsigned)(size_t)lds_;
  const unsigned tid = threadIdx.x, lane = tid & 63;
  const unsigned w = rfl(tid >> 6);
  const unsigned b = blockIdx.x, xcd = b & 7, r = b >> 3;
  STAMP(0);

  const __amdgpu_buffer_rsrc_t rsX = __builtin_amdgcn_make_buffer_rsrc((void*)x, (short)0, M_ * D_ * 2, 0x00020000);
  const __amdgpu_buffer_rsrc_t rsW = __builtin_amdgcn_make_buffer_rsrc((void*)(w < 4 ? wg : wu), (short)0, D_ * F_ * 2, 0x00020000);
  const __amdgpu_buffer_rsrc_t rsD = __builtin_amdgcn_make_buffer_rsrc((void*)wd, (short)0, F_ * D_ * 2, 0x00020000);
  const __amdgpu_buffer_rsrc_t rsWS = __builtin_amdgcn_make_buffer_rsrc((void*)ws, (short)0, WS_BYTES, 0x00020000);

  const unsigned pmi = r & 7, pfi = r >> 3;
  const unsigned pm = 8 * (xcd & 1) + pmi, pf = 4 * (xcd >> 1) + pfi;
  const bool producer = pf < 15;
  const unsigned f0 = pf < 14 ? 96 * pf : 1280;
  const unsigned rot1 = (pmi + 2 * pfi) & 7;
  const unsigned mt2 = 4 * xcd + (r >> 3), nt2 = r & 7;
  const unsigned kq = w & 3, mbl = w >> 2;
  const unsigned kb = kq == 0 ? 0 : kq == 1 ? 22 : kq == 2 ? 44 : 65;
  const unsigned nk = kq < 2 ? 22 : 21;
  const unsigned rot2 = (3 * nt2 + 5 * (r >> 3)) % 21;

  const unsigned voffX0 = (lane >> 3) * 1024 + (((lane & 7) ^ (lane >> 4)) << 4);
  const unsigned voffX1 = voffX0 ^ 64;
  const unsigned voffW = (lane >> 2) * WROW + (((lane & 3) ^ ((lane >> 5) << 1)) << 4);
  const unsigned voffD = (lane >> 3) * 1024 + (((lane & 7) ^ (((lane >> 4) & 1) << 2)) << 4);

  const unsigned sX = (pm * 128 + 16 * w) * 1024;
  const unsigned sW = ((w & 3) * 16) * WROW + f0 * 2;
  const unsigned mX = ldsb + 16 * w * 128;
  const unsigned mW = ldsb + WOFF + ((w >> 2) * 3 * 64 + (w & 3) * 16) * 64;

  auto dmaS = [&](unsigned s, int j) {
    const unsigned sl = (s + rot1) & 7, slot = (s & 3) * SLOT;
    if (j < 2) dma16(mX + slot + j * 1024, j ? voffX1 : voffX0, rsX, sX + j * 8192 + sl * 128);
    else dma16(mW + slot + (j - 2) * 4096, voffW, rsW, sW + sl * (64 * WROW) + (j - 2) * 64);
  };
  auto ks_of = [&](unsigned i) { unsigned t = i + rot2; t = t >= nk ? t - nk : t; return kb + t; };
  auto dmaG = [&](unsigned g, int j) {
    const unsigned uq = mbl * 5 + j, i = 5 * g + (uq >> 1), u = uq & 1;
    dma16(ldsb + (g * 40 + kq * 10 + uq) * 1024, voffD, rsD, (16 * ks_of(i) + 8 * u) * 1024 + nt2 * 128);
  };

  const v2u ep = __builtin_amdgcn_raw_buffer_load_b64(rsWS, 0, WS_EPOCH, AUX_SC1);

  if (producer) {
    const unsigned mi = w & 3, ni = w >> 2;
#pragma unroll
    for (int s = 0; s < 3; ++s)
#pragma unroll
      for (int j = 0; j < 5; ++j) dmaS(s, j);

    const unsigned xr0 = ldsb + (32 * mi + (lane & 15)) * 128 + ((((lane >> 4) ^ ((lane & 15) >> 1))) << 4);
    const unsigned xr1 = xr0 ^ 64;
    const unsigned g4 = lane >> 4, q = (lane & 15) >> 2, p = lane & 3;
    const unsigned a0 = (8 * g4 + q) * 64 + (g4 & 1) * 32 + 8 * p;
    unsigned wr[3];
#pragma unroll
    for (int j = 0; j < 3; ++j) { const unsigned ft = 3 * ni + j; wr[j] = ldsb + WOFF + (ft >> 1) * 4096 + (a0 ^ ((ft & 1) * 32)); }

    f32x4 acc[2][2][3];
#pragma unroll
    for (int a = 0; a < 2; ++a)
#pragma unroll
      for (int m = 0; m < 2; ++m)
#pragma unroll
        for (int j = 0; j < 3; ++j) acc[a][m][j] = f32x4{0.f, 0.f, 0.f, 0.f};

    WAIT_BAR(10);
    STAMP(1);
#pragma unroll 1
    for (unsigned s = 0; s < 8; ++s) {
      const unsigned slot = (s & 3) * SLOT;
      bf16x8 xf[2][2], wf[2][2][3];
#pragma unroll
      for (int ks = 0; ks < 2; ++ks) {
#pragma unroll
        for (int m = 0; m < 2; ++m) xf[ks][m] = *(LAS bf16x8*)((ks ? xr1 : xr0) + slot + m * 2048);
#pragma unroll
        for (int a = 0; a < 2; ++a)
#pragma unroll
          for (int j = 0; j < 3; ++j) {
            const unsigned ad = wr[j] + slot + a * 12288 + ks * 2048;
            wf[ks][a][j] = trfrag(ad, ad + 256);
          }
        if (s < 5) dmaS(s + 3, ks); else dmaG(s - 5, ks);
      }
#pragma unroll
      for (int ks = 0; ks < 2; ++ks) {
#pragma unroll
        for (int a = 0; a < 2; ++a) {
#pragma unroll
          for (int j = 0; j < 3; ++j)
#pragma unroll
            for (int m = 0; m < 2; ++m)
              acc[a][m][j] = __builtin_amdgcn_mfma_f32_16x16x32_bf16(wf[ks][a][j], xf[ks][m], acc[a][m][j], 0, 0, 0);
          __builtin_amdgcn_sched_barrier(0);
          if (ks * 2 + a < 3) { if (s < 5) dmaS(s + 3, 2 + ks * 2 + a); else dmaG(s - 5, 2 + ks * 2 + a); }
          __builtin_amdgcn_sched_barrier(0);
        }
      }
      if (s < 7) WAIT_V(10);
      asm volatile("s_waitcnt lgkmcnt(0)\n\ts_barrier" ::: "memory");
    }
    STAMP(2);
#pragma unroll
    for (int j = 0; j < 5; ++j) dmaG(3, j);
    const unsigned mb = 4 * pm + mi;
    const unsigned ks0 = f0 / 16 + 3 * ni;
#pragma unroll
    for (int j = 0; j < 3; ++j) {
      unsigned d[2][2];
#pragma unroll
      for (int m = 0; m < 2; ++m) {
        float h0 = silu_mul(acc[0][m][j][0], acc[1][m][j][0]);
        float h1 = silu_mul(acc[0][m][j][1], acc[1][m][j][1]);
        float h2 = silu_mul(acc[0][m][j][2], acc[1][m][j][2]);
        float h3 = silu_mul(acc[0][m][j][3], acc[1][m][j][3]);
        d[m][0] = cvtpk(h0, h1);
        d[m][1] = cvtpk(h2, h3);
      }
      auto s0 = __builtin_amdgcn_permlane16_swap(d[0][0], d[1][0], false, false);
      auto s1 = __builtin_amdgcn_permlane16_swap(d[0][1], d[1][1], false, false);
      v4u o = {s0[0], s1[0], s0[1], s1[1]};
      const unsigned ks = ks0 + j;
      if (pf < 14 || ks >= 84)
        __builtin_amdgcn_raw_buffer_store_b128(o, rsWS, lane * 16, WS_H + (mb * NKS + ks) * 1024, AUX_SC1);
    }
    STAMP(3);
    WAIT_V(0);
    STAMP(4);
    {
      unsigned long long e = ((unsigned long long)ep.y << 32 | ep.x) + 1ull;
      v2u tg = {(unsigned)e, (unsigned)(e >> 32)};
      if (lane == 0) __builtin_amdgcn_raw_buffer_store_b64(tg, rsWS, 0, WS_FLAGS + (mb * 32 + pf * 2 + ni) * 8, AUX_SC1);
    }
  } else {
#pragma unroll
    for (int g = 0; g < 4; ++g)
#pragma unroll
      for (int j = 0; j < 5; ++j) dmaG(g, j);
    WAIT_V(0);
    {
      const unsigned idx = ((xcd & 1) * 8 + pmi) * 8 + w;
      unsigned long long e = ((unsigned long long)ep.y << 32 | ep.x) + 1ull;
      v2u tg = {(unsigned)e, (unsigned)(e >> 32)};
      if (lane == 0) __builtin_amdgcn_raw_buffer_store_b64(tg, rsWS, 0, WS_FLAGS + ((idx >> 1) * 32 + 30 + (idx & 1)) * 8, AUX_SC1);
    }
  }
  const unsigned long long tag64 = ((unsigned long long)ep.y << 32 | ep.x) + 1ull;
  const unsigned tagx = (unsigned)tag64, tagy = (unsigned)(tag64 >> 32);

  asm volatile("s_barrier" ::: "memory");
  const unsigned hh = lane >> 5, blk = (lane >> 4) & 1, q2 = (lane & 15) >> 2, p2 = lane & 3;
  const unsigned dr0 = ldsb + kq * 10240 + hh * 1024 + q2 * 128 + ((q2 >> 1) * 64) + blk * 32 + 8 * p2;
  const unsigned dr1 = dr0 ^ 64;
  auto dpos = [&](int i) { const int ii = i >= 20 ? i - 20 : i; return (unsigned)((ii / 5) * 40960 + (ii % 5) * 2048); };
  bf16x8 pre[2][2];
#pragma unroll
  for (int i = 0; i < 2; ++i) {
    pre[i][0] = trfrag(dr0 + dpos(i), dr0 + dpos(i) + 512);
    pre[i][1] = trfrag(dr1 + dpos(i), dr1 + dpos(i) + 512);
  }
  asm volatile("s_waitcnt lgkmcnt(0)\n\ts_barrier" ::: "memory");
  {
    const unsigned i = 20 + mbl;
    if (i < nk) {
#pragma unroll
      for (int u = 0; u < 2; ++u)
        dma16(ldsb + (kq * 10 + mbl * 2 + u) * 1024, voffD, rsD, (16 * ks_of(i) + 8 * u) * 1024 + nt2 * 128);
    }
  }
  if (w == 7) {
    const unsigned off = WS_FLAGS + (2 * mt2 * 32 + lane) * 8;
    const bool need = (lane & 31) < 30;
    for (unsigned spin = 0; spin < (1u << 20); ++spin) {
      const v2u f = __builtin_amdgcn_raw_buffer_load_b64(rsWS, off, 0, AUX_SC1);
      const bool ok = !need || (f.x == tagx && f.y == tagy);
      if (__all(ok)) break;
      __builtin_amdgcn_s_sleep(1);
    }
    __builtin_amdgcn_fence(__ATOMIC_ACQUIRE, "wavefront");
  }
  asm volatile("s_barrier" ::: "memory");
  STAMP(5);
  v4u hf[22];
  {
    const unsigned hb = WS_H + ((2 * mt2 + mbl) * NKS) * 1024;
#pragma unroll
    for (int i = 0; i < 22; ++i) {
      const unsigned ii = (i < 21 || nk == 22) ? i : 0;
      hf[i] = __builtin_amdgcn_raw_buffer_load_b128(rsWS, lane * 16, hb + ks_of(ii) * 1024, AUX_SC1);
    }
  }
  __builtin_amdgcn_sched_barrier(0);
  WAIT_BAR(22);
  f32x16 acc2[2];
#pragma unroll
  for (int i = 0; i < 16; ++i) { acc2[0][i] = 0.f; acc2[1][i] = 0.f; }
#pragma unroll
  for (int i = 0; i < 22; ++i) {
    bf16x8 d0, d1;
    if (i < 2) { d0 = pre[i][0]; d1 = pre[i][1]; }
    else { d0 = trfrag(dr0 + dpos(i), dr0 + dpos(i) + 512); d1 = trfrag(dr1 + dpos(i), dr1 + dpos(i) + 512); }
    v4u hraw = hf[i];
    if (i == 21) { const unsigned msk = nk == 22 ? 0xffffffffu : 0u; hraw &= msk; }
    const bf16x8 hv = __builtin_bit_cast(bf16x8, hraw);
    acc2[0] = __builtin_amdgcn_mfma_f32_32x32x16_bf16(d0, hv, acc2[0], 0, 0, 0);
    acc2[1] = __builtin_amdgcn_mfma_f32_32x32x16_bf16(d1, hv, acc2[1], 0, 0, 0);
  }
  STAMP(6);
  asm volatile("s_waitcnt lgkmcnt(0)\n\ts_barrier" ::: "memory");
  {
    LAS f32x4* red = (LAS f32x4*)lds_;
#pragma unroll
    for (int d = 0; d < 4; ++d) {
      if ((unsigned)d != kq) {
#pragma unroll
        for (int g = 0; g < 2; ++g) {
          const int rb = 8 * (d & 1) + 4 * g;
          f32x4 v = {acc2[d >> 1][rb], acc2[d >> 1][rb + 1], acc2[d >> 1][rb + 2], acc2[d >> 1][rb + 3]};
          red[(((mbl * 4 + d) * 4 + kq) * 2 + g) * 64 + lane] = v;
        }
      }
    }
    asm volatile("s_waitcnt lgkmcnt(0)\n\ts_barrier" ::: "memory");
    f32x4 sum[2];
#pragma unroll
    for (int d = 0; d < 4; ++d)
      if ((unsigned)d == kq) {
#pragma unroll
        for (int g = 0; g < 2; ++g) {
          const int rb = 8 * (d & 1) + 4 * g;
          sum[g] = f32x4{acc2[d >> 1][rb], acc2[d >> 1][rb + 1], acc2[d >> 1][rb + 2], acc2[d >> 1][rb + 3]};
        }
      }
#pragma unroll
    for (int sidx = 1; sidx < 4; ++sidx) {
      const unsigned src = (kq + sidx) & 3;
#pragma unroll
      for (int g = 0; g < 2; ++g) sum[g] += red[(((mbl * 4 + kq) * 4 + src) * 2 + g) * 64 + lane];
    }
    unsigned a0_ = cvtpk(sum[0][0], sum[0][1]), a1_ = cvtpk(sum[0][2], sum[0][3]);
    unsigned b0_ = cvtpk(sum[1][0], sum[1][1]), b1_ = cvtpk(sum[1][2], sum[1][3]);
    auto t0 = __builtin_amdgcn_permlane32_swap(a0_, b0_, false, false);
    auto t1 = __builtin_amdgcn_permlane32_swap(a1_, b1_, false, false);
    v4u o = {t0[0], t1[0], t0[1], t1[1]};
    const unsigned row = 64 * mt2 + 32 * mbl + (lane & 31);
    const unsigned col = 64 * nt2 + 16 * kq + 8 * hh;
    *(GAS v4u*)(out + row * D_ + col) = o;
  }
  if (b == 0 && w == 0) {
    for (unsigned spin = 0; spin < (1u << 20); ++spin) {
      bool ok = true;
#pragma unroll
      for (int i = 0; i < 16; ++i) {
        const v4u f = __builtin_amdgcn_raw_buffer_load_b128(rsWS, lane * 16, WS_FLAGS + i * 1024, AUX_SC1);
        ok = ok && f.x == tagx && f.y == tagy && f.z == tagx && f.w == tagy;
      }
      if (__all(ok)) break;
      __builtin_amdgcn_s_sleep(1);
    }
    if (lane == 0) { v2u tg = {tagx, tagy}; __builtin_amdgcn_raw_buffer_store_b64(tg, rsWS, 0, WS_EPOCH, AUX_SC1); }
  }
  STAMP(7);
}

extern "C" void kernel_launch(void* const* d_in, const int* in_sizes, int n_in, void* d_out, int out_size, void* d_ws, size_t ws_size,
                              hipStream_t stream) {
  (void)in_sizes; (void)n_in; (void)out_size; (void)ws_size;
  hipFuncSetAttribute((const void*)ffn_fused, hipFuncAttributeMaxDynamicSharedMemorySize, 160 * 1024);
  ffn_fused<<<256, 512, 160 * 1024, stream>>>((const unsigned short*)d_in[0], (const unsigned short*)d_in[1], (const unsigned short*)d_in[2],
                                              (const unsigned short*)d_in[3], (unsigned short*)d_out, (unsigned char*)d_ws
                                              );
}
```

```cpp
#include <hip/hip_runtime.h>
#include <hip/hip_bf16.h>

typedef short bf16x8 __attribute__((ext_vector_type(8)));
typedef short s16x4 __attribute__((ext_vector_type(4)));
typedef float f32x4 __attribute__((ext_vector_type(4)));
typedef float f32x16 __attribute__((ext_vector_type(16)));
typedef float f32x2 __attribute__((ext_vector_type(2)));
typedef __bf16 bf16x2_t __attribute__((ext_vector_type(2)));
typedef unsigned v4u __attribute__((ext_vector_type(4)));
typedef unsigned v2u __attribute__((ext_vector_type(2)));
#define LAS __attribute__((address_space(3)))
#define GAS __attribute__((address_space(1)))
#define DI __device__ __forceinline__

constexpr int M_ = 2048, D_ = 512, F_ = 1376;
constexpr int WROW = F_ * 2;
constexpr int SLOT = 40960, XOFF = 24576, WHALF = 12288;
constexpr int NKS = F_ / 16;
constexpr int WS_EPOCH = 0;
constexpr int WS_FLAGS = 4096;
constexpr int WS_H = 65536;
constexpr int WS_BYTES = WS_H + 64 * NKS * 1024;
constexpr int AUX_SC1 = 16;
#define HPF 8

DI unsigned rfl(unsigned x) { return __builtin_amdgcn_readfirstlane(x); }
DI void dma16(unsigned m0v, unsigned voff, __amdgpu_buffer_rsrc_t rs, unsigned soff) {
  asm volatile("s_mov_b32 m0, %0\n\ts_nop 0\n\tbuffer_load_dwordx4 %1, %2, %3 offen lds"
               :: "s"(m0v), "v"(voff), "s"(rs), "s"(soff) : "memory");
}
DI unsigned cvtpk(float lo, float hi) {
  f32x2 v = {lo, hi};
  bf16x2_t b = __builtin_convertvector(v, bf16x2_t);
  return __builtin_bit_cast(unsigned, b);
}
DI bf16x8 trfrag(unsigned addr0, unsigned addr1) {
  s16x4 a = __builtin_amdgcn_ds_read_tr16_b64_v4i16((LAS s16x4*)addr0);
  s16x4 b = __builtin_amdgcn_ds_read_tr16_b64_v4i16((LAS s16x4*)addr1);
  return __builtin_shufflevector(a, b, 0, 1, 2, 3, 4, 5, 6, 7);
}
DI float silu_mul(float g, float u) {
  float e = __builtin_amdgcn_exp2f(g * -1.44269504088896340736f);
  return (g * u) * __builtin_amdgcn_rcpf(1.0f + e);
}
#define WAIT_V(N) asm volatile("s_waitcnt vmcnt(" #N ")" ::: "memory")
#define BAR() asm volatile("s_barrier" ::: "memory")
#define LBAR() asm volatile("s_waitcnt lgkmcnt(0)\n\ts_barrier" ::: "memory")
#define VBAR(N) asm volatile("s_waitcnt vmcnt(" #N ")\n\ts_barrier" ::: "memory")

#define STAMP(i)

struct Frags { bf16x8 x[4]; bf16x8 wv[2][3]; };

__global__ __launch_bounds__(512, 2) void ffn_fused(const unsigned short* __restrict__ x, const unsigned short* __restrict__ wg,
                                                    const unsigned short* __restrict__ wu, const unsigned short* __restrict__ wd,
                                                    unsigned short* __restrict__ out, unsigned char* __restrict__ ws
                                                    ) {
  extern __shared__ __attribute__((aligned(16))) char lds_[];
  const unsigned ldsb = (unsigned)(size_t)lds_;
  const unsigned tid = threadIdx.x, lane = tid & 63;
  const unsigned w = rfl(tid >> 6);
  const unsigned b = blockIdx.x, xcd = b & 7, r = b >> 3;
  STAMP(0);

  const __amdgpu_buffer_rsrc_t rsD = __builtin_amdgcn_make_buffer_rsrc((void*)wd, (short)0, F_ * D_ * 2, 0x00020000);
  const __amdgpu_buffer_rsrc_t rsWS = __builtin_amdgcn_make_buffer_rsrc((void*)ws, (short)0, WS_BYTES, 0x00020000);

  const unsigned pmi = r & 7, pfi = r >> 3;
  const unsigned pm = 8 * (xcd & 1) + pmi, pf = 4 * (xcd >> 1) + pfi;
  const bool producer = pf < 15;
  const unsigned f0 = pf < 14 ? 96 * pf : 1280;
  const unsigned mt2 = 4 * xcd + (r >> 3), nt2 = r & 7;
  const unsigned kq = w & 3, mbl = w >> 2;
  const unsigned kb = kq * 22 - (kq == 3 ? 1 : 0);
  const unsigned nk = kq < 2 ? 22 : 21;
  const unsigned rot2 = (3 * nt2 + 5 * (r >> 3)) % 21;
  auto ks_of = [&](unsigned i) { unsigned t = i + rot2; t = t >= nk ? t - nk : t; return kb + t; };
  const unsigned voffD = (lane >> 3) * 1024 + (((lane & 7) ^ (((lane >> 4) & 1) << 2)) << 4);
  auto dmaD = [&](unsigned u) {
    const unsigned up = u >= 40 ? u - 40 : u;
    dma16(ldsb + (up / 10) * SLOT + kq * 10240 + (up % 10) * 1024, voffD, rsD, (16 * ks_of(u >> 1) + 8 * (u & 1)) * 1024 + nt2 * 128);
  };

  unsigned tagx = 0, tagy = 0;

  if (w >= 4) {
    unsigned du = 0;
    if (producer) {
      const unsigned lw = w - 4;
      const __amdgpu_buffer_rsrc_t rsX = __builtin_amdgcn_make_buffer_rsrc((void*)x, (short)0, M_ * D_ * 2, 0x00020000);
      const __amdgpu_buffer_rsrc_t rsW = __builtin_amdgcn_make_buffer_rsrc((void*)(lw < 2 ? wg : wu), (short)0, D_ * F_ * 2, 0x00020000);
      const unsigned rot1 = (pmi + 2 * pfi) & 7;
      const unsigned voffX0 = (lane >> 3) * 1024 + (((lane & 7) ^ (lane >> 4)) << 4);
      const unsigned voffX1 = voffX0 ^ 64;
      const unsigned voffW = (lane >> 2) * WROW + (((lane & 3) ^ ((lane >> 5) << 1)) << 4);
      const unsigned sX = (pm * 128 + 32 * lw) * 1024;
      const unsigned sW = ((lw & 1) * 16) * WROW + f0 * 2;
      const unsigned mX = ldsb + XOFF + 32 * lw * 128;
      const unsigned mW = ldsb + (lw >> 1) * 6144 + (lw & 1) * 1024;
      auto dmaX = [&](unsigned j) {
        const unsigned sl = (j + rot1) & 7, slot = (j & 3) * SLOT;
#pragma unroll
        for (int i = 0; i < 4; ++i) dma16(mX + slot + i * 1024, (i & 1) ? voffX1 : voffX0, rsX, sX + i * 8192 + sl * 128);
      };
      auto dmaW = [&](unsigned j, unsigned h) {
        const unsigned sl = (j + rot1) & 7, slot = (j & 3) * SLOT + h * WHALF;
#pragma unroll
        for (int fb = 0; fb < 3; ++fb) dma16(mW + slot + fb * 2048, voffW, rsW, sW + (sl * 64 + h * 32) * WROW + fb * 64);
      };
      dmaW(0, 0); dmaX(0); dmaW(0, 1); dmaW(1, 0); dmaX(1);
#pragma unroll 1
      for (unsigned j = 0; j < 8; ++j) {
        VBAR(10);
        if (j < 7) dmaW(j + 1, 1);
        else { dmaD(7); dmaD(8); dmaD(9); }
        VBAR(10);
        if (j < 6) { dmaW(j + 2, 0); dmaX(j + 2); }
        else {
          const unsigned u0 = j == 6 ? 0 : 10;
#pragma unroll
          for (int i = 0; i < 7; ++i) dmaD(u0 + i);
        }
      }
      BAR();
      du = 17;
    }
#pragma unroll 1
    for (; du < 40; ++du) dmaD(du);
    STAMP(2);
    WAIT_V(0);
    STAMP(4);
  } else {
    const v2u ep = __builtin_amdgcn_raw_buffer_load_b64(rsWS, 0, WS_EPOCH, AUX_SC1);
    if (producer) {
      const unsigned mi = w & 1, ni = w >> 1;
      const unsigned xr0 = ldsb + XOFF + (64 * mi + (lane & 15)) * 128 + ((((lane >> 4) ^ ((lane & 15) >> 1))) << 4);
      const unsigned xr1 = xr0 ^ 64;
      const unsigned g4 = lane >> 4, q = (lane & 15) >> 2, p = lane & 3;
      const unsigned a0 = (8 * g4 + q) * 64 + (g4 & 1) * 32 + 8 * p;
      unsigned wr[3];
#pragma unroll
      for (int j = 0; j < 3; ++j) { const unsigned ft = 3 * ni + j; wr[j] = ldsb + (ft >> 1) * 2048 + (a0 ^ ((ft & 1) * 32)); }

      f32x4 acc[2][4][3];
#pragma unroll
      for (int a = 0; a < 2; ++a)
#pragma unroll
        for (int m = 0; m < 4; ++m)
#pragma unroll
          for (int j = 0; j < 3; ++j) acc[a][m][j] = f32x4{0.f, 0.f, 0.f, 0.f};

      auto rd = [&](Frags& Fr, unsigned slot, int h) {
#pragma unroll
        for (int m = 0; m < 4; ++m) Fr.x[m] = *(LAS bf16x8*)((h ? xr1 : xr0) + slot + m * 2048);
#pragma unroll
        for (int a = 0; a < 2; ++a)
#pragma unroll
          for (int j = 0; j < 3; ++j) {
            const unsigned ad = wr[j] + slot + h * WHALF + a * 6144;
            Fr.wv[a][j] = trfrag(ad, ad + 256);
          }
      };
      auto mm = [&](const Frags& Fr, int a) {
#pragma unroll
        for (int j = 0; j < 3; ++j)
#pragma unroll
          for (int m = 0; m < 4; ++m)
            acc[a][m][j] = __builtin_amdgcn_mfma_f32_16x16x32_bf16(Fr.wv[a][j], Fr.x[m], acc[a][m][j], 0, 0, 0);
      };
      auto interleave = [&]() {
#pragma unroll
        for (int i = 0; i < 8; ++i) {
          __builtin_amdgcn_sched_group_barrier(0x100, 2, 0);
          __builtin_amdgcn_sched_group_barrier(0x008, 1, 0);
        }
        __builtin_amdgcn_sched_group_barrier(0x008, 4, 0);
      };
      Frags A, B;
      BAR();
      STAMP(1);
      rd(A, 0, 0);
#pragma unroll 1
      for (unsigned j = 0; j < 8; ++j) {
        const unsigned slot = (j & 3) * SLOT, slotn = ((j + 1) & 3) * SLOT;
        mm(A, 0);
        __builtin_amdgcn_sched_barrier(0);
        LBAR();
        rd(B, slot, 1);
        mm(A, 1);
        interleave();
        __builtin_amdgcn_sched_barrier(0);
        mm(B, 0);
        __builtin_amdgcn_sched_barrier(0);
        LBAR();
        rd(A, slotn, 0);
        mm(B, 1);
        interleave();
        __builtin_amdgcn_sched_barrier(0);
      }
      STAMP(2);
      const unsigned ks0 = f0 / 16 + 3 * ni;
#pragma unroll
      for (int mp = 0; mp < 2; ++mp) {
        const unsigned mb = 4 * pm + 2 * mi + mp;
#pragma unroll
        for (int j = 0; j < 3; ++j) {
          unsigned d[2][2];
#pragma unroll
          for (int m = 0; m < 2; ++m) {
            const f32x4 g = acc[0][2 * mp + m][j], u = acc[1][2 * mp + m][j];
            d[m][0] = cvtpk(silu_mul(g[0], u[0]), silu_mul(g[1], u[1]));
            d[m][1] = cvtpk(silu_mul(g[2], u[2]), silu_mul(g[3], u[3]));
          }
          auto s0 = __builtin_amdgcn_permlane16_swap(d[0][0], d[1][0], false, false);
          auto s1 = __builtin_amdgcn_permlane16_swap(d[0][1], d[1][1], false, false);
          v4u o = {s0[0], s1[0], s0[1], s1[1]};
          const unsigned ks = ks0 + j;
          if (pf < 14 || ks >= 84)
            __builtin_amdgcn_raw_buffer_store_b128(o, rsWS, lane * 16, WS_H + (mb * NKS + ks) * 1024, AUX_SC1);
        }
      }
      STAMP(3);
      WAIT_V(0);
      STAMP(4);
    }
    {
      const unsigned long long e = ((unsigned long long)ep.y << 32 | ep.x) + 1ull;
      tagx = (unsigned)e; tagy = (unsigned)(e >> 32);
      const v2u tg = {tagx, tagy};
      const unsigned fidx = producer ? (2 * pm + (w & 1)) * 32 + pf * 2 + (w >> 1) : ((xcd & 1) * 8 + pmi) * 32 + 30;
      if (lane == 0 && (producer || w == 0)) __builtin_amdgcn_raw_buffer_store_b64(tg, rsWS, 0, WS_FLAGS + fidx * 8, AUX_SC1);
    }
  }

  BAR();
  const unsigned hh = lane >> 5, blk = (lane >> 4) & 1, q2 = (lane & 15) >> 2, p2 = lane & 3;
  const unsigned dr0 = ldsb + kq * 10240 + hh * 1024 + q2 * 128 + ((q2 >> 1) * 64) + blk * 32 + 8 * p2;
  const unsigned dr1 = dr0 ^ 64;
  auto dpos = [&](int i) { const int ii = i >= 20 ? i - 20 : i; return (unsigned)((ii / 5) * SLOT + (ii % 5) * 2048); };
  bf16x8 pre[2][2];
#pragma unroll
  for (int i = 0; i < 2; ++i) {
    pre[i][0] = trfrag(dr0 + dpos(i), dr0 + dpos(i) + 512);
    pre[i][1] = trfrag(dr1 + dpos(i), dr1 + dpos(i) + 512);
  }
  LBAR();
  if (w >= 4) {
    dmaD(40); dmaD(41);
    if (nk == 22) { dmaD(42); dmaD(43); }
    WAIT_V(0);
  } else if (w == 0) {
    const unsigned off = WS_FLAGS + (mt2 * 32 + (lane & 31)) * 8;
    const bool need = (lane & 31) < 30;
    for (unsigned spin = 0; spin < (1u << 20); ++spin) {
      const v2u f = __builtin_amdgcn_raw_buffer_load_b64(rsWS, off, 0, AUX_SC1);
      const bool ok = !need || (f.x == tagx && f.y == tagy);
      if (__all(ok)) break;
      __builtin_amdgcn_s_sleep(1);
    }
    __builtin_amdgcn_fence(__ATOMIC_ACQUIRE, "wavefront");
  }
  BAR();
  STAMP(5);
  f32x16 acc2[2];
#pragma unroll
  for (int i = 0; i < 16; ++i) { acc2[0][i] = 0.f; acc2[1][i] = 0.f; }
  {
    const unsigned hb = WS_H + ((2 * mt2 + mbl) * NKS) * 1024;
    v4u hf[HPF];
    auto ldh = [&](int i) {
      const unsigned ii = (i < 21 || nk == 22) ? i : 0;
      return __builtin_amdgcn_raw_buffer_load_b128(rsWS, lane * 16, hb + ks_of(ii) * 1024, AUX_SC1);
    };
#pragma unroll
    for (int i = 0; i < HPF; ++i) hf[i] = ldh(i);
    bf16x8 dn0 = pre[0][0], dn1 = pre[0][1];
#pragma unroll
    for (int i = 0; i < 22; ++i) {
      __builtin_amdgcn_sched_barrier(0);
      const bf16x8 d0 = dn0, d1 = dn1;
      if (i + 1 < 2) { dn0 = pre[i + 1][0]; dn1 = pre[i + 1][1]; }
      else if (i + 1 < 22) { dn0 = trfrag(dr0 + dpos(i + 1), dr0 + dpos(i + 1) + 512); dn1 = trfrag(dr1 + dpos(i + 1), dr1 + dpos(i + 1) + 512); }
      v4u hraw = hf[i % HPF];
      if (i == 21) { const unsigned msk = nk == 22 ? 0xffffffffu : 0u; hraw &= msk; }
      const bf16x8 hv = __builtin_bit_cast(bf16x8, hraw);
      acc2[0] = __builtin_amdgcn_mfma_f32_32x32x16_bf16(d0, hv, acc2[0], 0, 0, 0);
      acc2[1] = __builtin_amdgcn_mfma_f32_32x32x16_bf16(d1, hv, acc2[1], 0, 0, 0);
      if (i + HPF < 22) hf[i % HPF] = ldh(i + HPF);
    }
    __builtin_amdgcn_sched_barrier(0);
  }
  STAMP(6);
  LBAR();
  {
    LAS f32x4* red = (LAS f32x4*)lds_;
#pragma unroll
    for (int d = 0; d < 4; ++d) {
      if ((unsigned)d != kq) {
#pragma unroll
        for (int g = 0; g < 2; ++g) {
          const int rb = 8 * (d & 1) + 4 * g;
          f32x4 v = {acc2[d >> 1][rb], acc2[d >> 1][rb + 1], acc2[d >> 1][rb + 2], acc2[d >> 1][rb + 3]};
          red[(((mbl * 4 + d) * 4 + kq) * 2 + g) * 64 + lane] = v;
        }
      }
    }
    LBAR();
    f32x4 sum[2];
#pragma unroll
    for (int d = 0; d < 4; ++d)
      if ((unsigned)d == kq) {
#pragma unroll
        for (int g = 0; g < 2; ++g) {
          const int rb = 8 * (d & 1) + 4 * g;
          sum[g] = f32x4{acc2[d >> 1][rb], acc2[d >> 1][rb + 1], acc2[d >> 1][rb + 2], acc2[d >> 1][rb + 3]};
        }
      }
#pragma unroll
    for (int sidx = 1; sidx < 4; ++sidx) {
      const unsigned src = (kq + sidx) & 3;
#pragma unroll
      for (int g = 0; g < 2; ++g) sum[g] += red[(((mbl * 4 + kq) * 4 + src) * 2 + g) * 64 + lane];
    }
    unsigned a0_ = cvtpk(sum[0][0], sum[0][1]), a1_ = cvtpk(sum[0][2], sum[0][3]);
    unsigned b0_ = cvtpk(sum[1][0], sum[1][1]), b1_ = cvtpk(sum[1][2], sum[1][3]);
    auto t0 = __builtin_amdgcn_permlane32_swap(a0_, b0_, false, false);
    auto t1 = __builtin_amdgcn_permlane32_swap(a1_, b1_, false, false);
    v4u o = {t0[0], t1[0], t0[1], t1[1]};
    const unsigned row = 64 * mt2 + 32 * mbl + (lane & 31);
    const unsigned col = 64 * nt2 + 16 * kq + 8 * hh;
    *(GAS v4u*)(out + row * D_ + col) = o;
  }
  if (b == 0 && w == 0) {
    for (unsigned spin = 0; spin < (1u << 20); ++spin) {
      bool ok = true;
#pragma unroll
      for (int i = 0; i < 8; ++i) {
        const v4u f = __builtin_amdgcn_raw_buffer_load_b128(rsWS, lane * 16, WS_FLAGS + i * 1024, AUX_SC1);
        const bool last = (lane & 15) == 15;
        const bool lo_ok = f.x == tagx && f.y == tagy, hi_ok = f.z == tagx && f.w == tagy;
        ok = ok && (last ? (i >= 4 || lo_ok) : (lo_ok && hi_ok));
      }
      if (__all(ok)) break;
      __builtin_amdgcn_s_sleep(1);
    }
    if (lane == 0) { v2u tg = {tagx, tagy}; __builtin_amdgcn_raw_buffer_store_b64(tg, rsWS, 0, WS_EPOCH, AUX_SC1); }
  }
  STAMP(7);
}

extern "C" void kernel_launch(void* const* d_in, const int* in_sizes, int n_in, void* d_out, int out_size, void* d_ws, size_t ws_size,
                              hipStream_t stream) {
  (void)in_sizes; (void)n_in; (void)out_size; (void)ws_size;
  (void)hipFuncSetAttribute((const void*)ffn_fused, hipFuncAttributeMaxDynamicSharedMemorySize, 160 * 1024);
  ffn_fused<<<256, 512, 160 * 1024, stream>>>((const unsigned short*)d_in[0], (const unsigned short*)d_in[1], (const unsigned short*)d_in[2],
                                              (const unsigned short*)d_in[3], (unsigned short*)d_out, (unsigned char*)d_ws
                                              );
}
```

```cpp
#include <hip/hip_runtime.h>
#include <hip/hip_bf16.h>

typedef short bf16x8 __attribute__((ext_vector_type(8)));
typedef short s16x4 __attribute__((ext_vector_type(4)));
typedef float f32x4 __attribute__((ext_vector_type(4)));
typedef float f32x16 __attribute__((ext_vector_type(16)));
typedef float f32x2 __attribute__((ext_vector_type(2)));
typedef __bf16 bf16x2_t __attribute__((ext_vector_type(2)));
typedef unsigned v4u __attribute__((ext_vector_type(4)));
typedef unsigned v2u __attribute__((ext_vector_type(2)));
#define LAS __attribute__((address_space(3)))
#define GAS __attribute__((address_space(1)))
#define DI __device__ __forceinline__

constexpr int M_ = 2048, D_ = 512, F_ = 1376;
constexpr int WROW = F_ * 2;
constexpr int SLOT = 40960, XOFF = 24576, WHALF = 12288;
constexpr int NKS = F_ / 16;
constexpr int WS_EPOCH = 0;
constexpr int WS_DONE = 1024;
constexpr int WS_FLAGS = 4096;
constexpr int WS_H = 65536;
constexpr int WS_BYTES = WS_H + 64 * NKS * 1024;
constexpr int AUX_SC1 = 16;
#define HPF 7
#define ROT1 2
#define ROT2 1
#define CODEPF 1
#define DPART1 25

DI unsigned rfl(unsigned x) { return __builtin_amdgcn_readfirstlane(x); }
DI void dma16(unsigned m0v, unsigned voff, __amdgpu_buffer_rsrc_t rs, unsigned soff) {
  asm volatile("s_mov_b32 m0, %0\n\ts_nop 0\n\tbuffer_load_dwordx4 %1, %2, %3 offen lds"
               :: "s"(m0v), "v"(voff), "s"(rs), "s"(soff) : "memory");
}
DI unsigned cvtpk(f32x2 v) {
  bf16x2_t b = __builtin_convertvector(v, bf16x2_t);
  return __builtin_bit_cast(unsigned, b);
}
DI bf16x8 trfrag(unsigned addr0, unsigned addr1) {
  s16x4 a = __builtin_amdgcn_ds_read_tr16_b64_v4i16((LAS s16x4*)addr0);
  s16x4 b = __builtin_amdgcn_ds_read_tr16_b64_v4i16((LAS s16x4*)addr1);
  return __builtin_shufflevector(a, b, 0, 1, 2, 3, 4, 5, 6, 7);
}
DI unsigned silu_mul2(f32x2 g, f32x2 u) {
  const f32x2 t = g * -1.44269504088896340736f;
  f32x2 e = {__builtin_amdgcn_exp2f(t.x), __builtin_amdgcn_exp2f(t.y)};
  e = e + 1.0f;
  const f32x2 rc = {__builtin_amdgcn_rcpf(e.x), __builtin_amdgcn_rcpf(e.y)};
  return cvtpk((g * u) * rc);
}
#define WAIT_V(N) asm volatile("s_waitcnt vmcnt(" #N ")" ::: "memory")
#define BAR() asm volatile("s_barrier" ::: "memory")
#define LBAR() asm volatile("s_waitcnt lgkmcnt(0)\n\ts_barrier" ::: "memory")
#define VBAR(N) asm volatile("s_waitcnt vmcnt(" #N ")\n\ts_barrier" ::: "memory")

#define STAMP(i)

struct Frags { bf16x8 x; bf16x8 wv[2][3]; };

__global__ __launch_bounds__(512, 2) void ffn_fused(const unsigned short* __restrict__ x, const unsigned short* __restrict__ wg,
                                                    const unsigned short* __restrict__ wu, const unsigned short* __restrict__ wd,
                                                    unsigned short* __restrict__ out, unsigned char* __restrict__ ws
                                                    ) {
  extern __shared__ __attribute__((aligned(16))) char lds_[];
  const unsigned ldsb = (unsigned)(size_t)lds_;
  const unsigned tid = threadIdx.x, lane = tid & 63;
  const unsigned w = rfl(tid >> 6);
  const unsigned b = blockIdx.x, xcd = b & 7, r = b >> 3;
  STAMP(0);

  const unsigned pmi = r & 7, pfi = r >> 3;
  const unsigned pm = 8 * (xcd & 1) + pmi, pf = 4 * (xcd >> 1) + pfi;
  const bool producer = pf < 15;
  const unsigned f0 = pf < 14 ? 96 * pf : 1280;
  const bool loader = w < 4;
  const unsigned kq = w & 3, mbl = w >> 2;

  unsigned long long px = (unsigned long long)x, pw = (unsigned long long)(kq < 2 ? wg : wu), pd = (unsigned long long)wd,
                     po = (unsigned long long)out, pws = (unsigned long long)ws;
  asm volatile("" : "+s"(px), "+s"(pw), "+s"(pd), "+s"(po), "+s"(pws));

  unsigned long long epoch = *(const __attribute__((address_space(4))) unsigned long long*)(pws + WS_EPOCH);

  if (loader && producer) {
    const unsigned lw = w;
    const __amdgpu_buffer_rsrc_t rsX = __builtin_amdgcn_make_buffer_rsrc((void*)px, (short)0, M_ * D_ * 2, 0x00020000);
    const __amdgpu_buffer_rsrc_t rsW = __builtin_amdgcn_make_buffer_rsrc((void*)pw, (short)0, D_ * F_ * 2, 0x00020000);
    const unsigned rot1 = ROT1 == 1 ? ((pmi + 2 * pfi) & 7) : ROT1 == 2 ? ((2 * (xcd >> 1) + 4 * (xcd & 1)) & 7) : 0;
    const unsigned voffX0 = (lane >> 3) * 1024 + (((lane & 7) ^ (lane >> 4)) << 4);
    const unsigned voffX1 = voffX0 ^ 64;
    const unsigned voffW = (lane >> 2) * WROW + ((lane & 3) << 4);
    const unsigned sX = (pm * 128 + 16 * lw) * 1024;
    const unsigned sW = ((lw & 1) * 16) * WROW + f0 * 2;
    const unsigned mX = ldsb + XOFF + 16 * lw * 128;
    const unsigned mW = ldsb + (lw >> 1) * 6144 + (lw & 1) * 1024;
    auto dmaX = [&](unsigned xh) {
      const unsigned j = xh >> 1, h = xh & 1, sl = (j + rot1) & 7;
      const unsigned m0 = mX + (j & 3) * SLOT + h * 8192, so = sX + h * 65536 + sl * 128;
      dma16(m0, voffX0, rsX, so);
      dma16(m0 + 1024, voffX1, rsX, so + 8192);
    };
    auto dmaW = [&](unsigned p) {
      const unsigned j = p >> 1, h = p & 1, sl = (j + rot1) & 7;
      const unsigned m0 = mW + (j & 3) * SLOT + h * WHALF, so = sW + (sl * 64 + h * 32) * WROW;
#pragma unroll
      for (int fb = 0; fb < 3; ++fb) dma16(m0 + fb * 2048, voffW, rsW, so + fb * 64);
    };
    dmaW(0); dmaX(0); dmaX(1);
    dmaW(1); dmaX(2);
    dmaW(2); dmaX(3);
    asm volatile("" :: "s"(epoch));
    STAMP(1);
#pragma unroll 1
    for (unsigned p = 0; p < 12; ++p) {
      VBAR(10);
      dmaW(p + 3);
      dmaX(p + 4);
    }
    VBAR(10);
    dmaW(15);
  }

  const __amdgpu_buffer_rsrc_t rsD = __builtin_amdgcn_make_buffer_rsrc((void*)pd, (short)0, F_ * D_ * 2, 0x00020000);
  const __amdgpu_buffer_rsrc_t rsWS = __builtin_amdgcn_make_buffer_rsrc((void*)pws, (short)0, WS_BYTES, 0x00020000);
  const unsigned mt2 = 4 * xcd + (r >> 3), nt2 = r & 7;
  const unsigned nk = kq < 2 ? 22 : 21;
  const unsigned e2b = kq * 8 - (kq == 3 ? 1 : 0);
  auto ks_of = [&](unsigned i) {
    const unsigned part = i < 7 ? 0 : i < 14 ? 1 : 2;
    const unsigned e = part < 2 ? 7 * kq + i - 7 * part : e2b + i - 14;
    const unsigned ks = 6 * (e >> 1) + 2 * part + (e & 1);
    return ks >= 88 ? ks - 4 : ks;
  };
  const unsigned voffD = (lane >> 3) * 1024 + (((lane & 7) ^ (((lane >> 4) & 1) << 2)) << 4);
  auto dmaD = [&](unsigned u) {
    const unsigned up = u >= 40 ? u - 40 : u;
    dma16(ldsb + (up / 10) * SLOT + kq * 10240 + (up % 10) * 1024, voffD, rsD, (16 * ks_of(u >> 1) + 8 * (u & 1)) * 1024 + nt2 * 128);
  };

  asm volatile("" : "+s"(epoch));
  const unsigned long long tag64 = epoch + 1ull;
  const unsigned tagx = (unsigned)tag64, tagy = (unsigned)(tag64 >> 32);

  if (loader) {
    unsigned du = 0;
    if (producer) {
      dmaD(0); dmaD(1);
      VBAR(10);
#pragma unroll
      for (int i = 2; i < 7; ++i) dmaD(i);
      VBAR(10);
#pragma unroll
      for (int i = 7; i < 12; ++i) dmaD(i);
      VBAR(10);
#pragma unroll
      for (int i = 12; i < 17; ++i) dmaD(i);
      BAR();
      STAMP(2);
      du = 17;
    }
#pragma unroll 1
    for (; du < DPART1; ++du) dmaD(du);
    asm volatile("s_waitcnt vmcnt(%0)\n\ts_barrier" :: "n"(DPART1 - 14) : "memory");
    STAMP(3);
#pragma unroll 1
    for (; du < 40; ++du) dmaD(du);
    STAMP(4);
  } else {
    unsigned cpf_v = 0;
    if (w == 4 && producer) {
      unsigned long long pc;
      unsigned len;
      asm volatile("s_getpc_b64 %0\n.Lffn_pf_here:\n\ts_mov_b32 %1, .Lffn_pf_end-.Lffn_pf_here" : "=s"(pc), "=s"(len));
      const __amdgpu_buffer_rsrc_t rsC = __builtin_amdgcn_make_buffer_rsrc((void*)pc, (short)0, (int)len, 0x00020000);
      if (lane < 4) cpf_v = __builtin_amdgcn_raw_buffer_load_b32(rsC, r * 512 + lane * 128, 0, 0);
    }
    if (producer) {
      const unsigned mbw = w & 3;
      const unsigned r32 = lane & 31, h2 = lane >> 5;
      unsigned xr[4];
#pragma unroll
      for (int c2 = 0; c2 < 4; ++c2) xr[c2] = ldsb + XOFF + (32 * mbw + r32) * 128 + (((2 * c2 + h2) ^ ((r32 >> 1) & 7)) << 4);
      const unsigned wr = ldsb + (8 * h2 + ((lane & 15) >> 2)) * 64 + ((lane >> 4) & 1) * 32 + 8 * (lane & 3);

      f32x16 acc[2][3];
#pragma unroll
      for (int a = 0; a < 2; ++a)
#pragma unroll
        for (int j = 0; j < 3; ++j) {
#pragma unroll
          for (int i = 0; i < 16; ++i) acc[a][j][i] = 0.f;
          asm volatile("" : "+v"(acc[a][j]));
        }
      auto rd = [&](Frags& Fr, unsigned slot, int h, int k) {
        Fr.x = *(LAS bf16x8*)(xr[2 * h + k] + slot);
#pragma unroll
        for (int a = 0; a < 2; ++a)
#pragma unroll
          for (int j = 0; j < 3; ++j) {
            const unsigned ad = wr + slot + h * WHALF + a * 6144 + j * 2048 + k * 1024;
            Fr.wv[a][j] = trfrag(ad, ad + 256);
          }
      };
      auto mm = [&](const Frags& Fr) {
#pragma unroll
        for (int a = 0; a < 2; ++a)
#pragma unroll
          for (int j = 0; j < 3; ++j) acc[a][j] = __builtin_amdgcn_mfma_f32_32x32x16_bf16(Fr.wv[a][j], Fr.x, acc[a][j], 0, 0, 0);
      };
      auto il = [&]() {
        __builtin_amdgcn_sched_group_barrier(0x008, 1, 0);
        __builtin_amdgcn_sched_group_barrier(0x100, 3, 0);
#pragma unroll
        for (int i = 0; i < 5; ++i) {
          __builtin_amdgcn_sched_group_barrier(0x008, 1, 0);
          __builtin_amdgcn_sched_group_barrier(0x100, 2, 0);
        }
        __builtin_amdgcn_sched_barrier(0);
      };
      Frags A, B;
      __builtin_amdgcn_s_setprio(1);
      BAR();
      STAMP(1);
      rd(A, 0, 0, 0);
#pragma unroll 1
      for (unsigned j = 0; j < 8; ++j) {
        const unsigned slot = (j & 3) * SLOT, slotn = ((j + 1) & 3) * SLOT;
        rd(B, slot, 0, 1); mm(A); il();
        BAR();
        rd(A, slot, 1, 0); mm(B); il();
        rd(B, slot, 1, 1); mm(A); il();
        if (j == 7) asm volatile("s_waitcnt lgkmcnt(0)" ::: "memory");
        BAR();
        rd(A, slotn, 0, 0);
        mm(B); il();
      }
      STAMP(2);
      asm volatile("" :: "v"(cpf_v));
      const unsigned mb = 4 * pm + mbw;
#pragma unroll
      for (int j = 0; j < 3; ++j) {
#pragma unroll
        for (int sx = 0; sx < 2; ++sx) {
          v4u o;
#pragma unroll
          for (int e = 0; e < 4; ++e) {
            const int i0 = 8 * sx + 2 * e;
            o[e] = silu_mul2(f32x2{acc[0][j][i0], acc[0][j][i0 + 1]}, f32x2{acc[1][j][i0], acc[1][j][i0 + 1]});
          }
          const unsigned ks = f0 / 16 + 2 * j + sx;
          if (pf < 14 || j == 2)
            __builtin_amdgcn_raw_buffer_store_b128(o, rsWS, lane * 16 + WS_H + (mb * NKS + ks) * 1024, 0, AUX_SC1);
        }
      }
      __builtin_amdgcn_s_setprio(0);
    }
    BAR();
    STAMP(3);
    {
      const v2u tg = {tagx, tagy};
      if (producer) {
        const unsigned fo = WS_FLAGS + ((4 * pm + (w & 3)) * 48 + pf) * 8;
        if (pf < 14) {
          WAIT_V(4);
          if (lane == 0) __builtin_amdgcn_raw_buffer_store_b64(tg, rsWS, 0, fo, AUX_SC1);
          WAIT_V(3);
          if (lane == 0) __builtin_amdgcn_raw_buffer_store_b64(tg, rsWS, 0, fo + 128, AUX_SC1);
          WAIT_V(2);
        } else {
          WAIT_V(0);
        }
        if (lane == 0) __builtin_amdgcn_raw_buffer_store_b64(tg, rsWS, 0, fo + 256, AUX_SC1);
      } else if (w == 4 && lane == 0) {
        __builtin_amdgcn_raw_buffer_store_b64(tg, rsWS, 0, WS_DONE + (32 + (xcd & 1) * 8 + pmi) * 8, AUX_SC1);
      }
    }
    STAMP(4);
  }
  const unsigned hh = lane >> 5, blk = (lane >> 4) & 1, q2 = (lane & 15) >> 2, p2 = lane & 3;
  const unsigned dr0 = ldsb + kq * 10240 + (4 * hh + q2) * 128 + ((q2 >> 1) * 64) + blk * 32 + 8 * p2;
  const unsigned dr1 = dr0 ^ 64;
  auto dpos = [&](int i) { const int ii = i >= 20 ? i - 20 : i; return (unsigned)((ii / 5) * SLOT + (ii % 5) * 2048); };
  f32x16 acc2[2];
#pragma unroll
  for (int i = 0; i < 16; ++i) { acc2[0][i] = 0.f; acc2[1][i] = 0.f; }
  asm volatile("" : "+v"(acc2[0]), "+v"(acc2[1]));
  v2u done = {0u, 0u};
  {
    const unsigned mb2 = 2 * mt2 + mbl;
    const unsigned hb = WS_H + (mb2 * NKS) * 1024;
    const unsigned foff = WS_FLAGS + (mb2 * 48 + (lane < 48 ? lane : 0)) * 8;
    const unsigned lpf = lane & 15, lpart = lane >> 4;
    const unsigned lo01 = (7 * kq) >> 1, hi01 = (7 * kq + 6) >> 1, lo2 = e2b >> 1, hi2 = (e2b + nk - 15) >> 1;
    const bool mine = lpart < 2 ? (lpf >= lo01 && lpf <= hi01) : (lpart == 2 && lpf >= lo2 && lpf <= hi2);
    unsigned long long seen = 0;
    auto wait_part = [&](int part) {
      const unsigned long long need = __builtin_amdgcn_ballot_w64(mine && lpart == (unsigned)part);
      for (unsigned spin = 0; (seen & need) != need && spin < (1u << 20); ++spin) {
        const v2u f = __builtin_amdgcn_raw_buffer_load_b64(rsWS, foff, 0, AUX_SC1);
        seen = __builtin_amdgcn_ballot_w64(f.x == tagx && f.y == tagy);
        if ((seen & need) != need) __builtin_amdgcn_s_sleep(1);
      }
      __builtin_amdgcn_fence(__ATOMIC_ACQUIRE, "wavefront");
    };
    v4u hf[HPF];
    auto ldh = [&](int i) {
      if (i == 0) wait_part(0);
      if (i == 7) wait_part(1);
      if (i == 14) wait_part(2);
      const unsigned ii = (i < 21 || nk == 22) ? i : 0;
      return __builtin_amdgcn_raw_buffer_load_b128(rsWS, lane * 16, rfl(hb + ks_of(ii) * 1024), AUX_SC1);
    };
    bf16x8 dn0 = trfrag(dr0, dr0 + 1024), dn1 = trfrag(dr1, dr1 + 1024);
#pragma unroll
    for (int i = 0; i < HPF; ++i) { hf[i] = ldh(i); if (i == 0) { STAMP(5); } }
#pragma unroll
    for (int i = 0; i < 22; ++i) {
      __builtin_amdgcn_sched_barrier(0);
      if (i == 6) {
        LBAR();
        if (loader) {
          dmaD(40); dmaD(41);
          if (nk == 22) { dmaD(42); dmaD(43); }
        }
      }
      if (i == 6 + HPF + 1) LBAR();
      const bf16x8 d0 = dn0, d1 = dn1;
      if (i + 1 < 22) { dn0 = trfrag(dr0 + dpos(i + 1), dr0 + dpos(i + 1) + 1024); dn1 = trfrag(dr1 + dpos(i + 1), dr1 + dpos(i + 1) + 1024); }
      v4u hraw = hf[i % HPF];
      if (i == 21) { const unsigned msk = nk == 22 ? 0xffffffffu : 0u; hraw &= msk; }
      const bf16x8 hv = __builtin_bit_cast(bf16x8, hraw);
      acc2[0] = __builtin_amdgcn_mfma_f32_32x32x16_bf16(d0, hv, acc2[0], 0, 0, 0);
      acc2[1] = __builtin_amdgcn_mfma_f32_32x32x16_bf16(d1, hv, acc2[1], 0, 0, 0);
      if (i + HPF < 22) hf[i % HPF] = ldh(i + HPF);
      if (i == 6 + HPF + 1 && nt2 == 0 && w == 4 && lane == 0) {
        const v2u tg = {tagx, tagy};
        __builtin_amdgcn_raw_buffer_store_b64(tg, rsWS, 0, WS_DONE + mt2 * 8, AUX_SC1);
      }
    }
    __builtin_amdgcn_sched_barrier(0);
  }
  if (b == 0 && w == 4) done = __builtin_amdgcn_raw_buffer_load_b64(rsWS, lane * 8, WS_DONE, AUX_SC1);
  STAMP(6);
  {
    auto piece = [&](unsigned id) { return ldsb + (id < 20 ? SLOT + id * 2048 : 2 * SLOT + (id - 20) * 10240) + lane * 16; };
#pragma unroll
    for (int d = 0; d < 4; ++d) {
      if ((unsigned)d != kq) {
        const unsigned k = (kq - d - 1) & 3;
        const unsigned ad = piece((mbl * 4 + d) * 3 + k);
#pragma unroll
        for (int g = 0; g < 2; ++g) {
          const int rb = 8 * (d & 1) + 4 * g;
          f32x4 v = {acc2[d >> 1][rb], acc2[d >> 1][rb + 1], acc2[d >> 1][rb + 2], acc2[d >> 1][rb + 3]};
          *(LAS f32x4*)(ad + g * 1024) = v;
        }
      }
    }
    LBAR();
    f32x4 sum[2];
#pragma unroll
    for (int d = 0; d < 4; ++d)
      if ((unsigned)d == kq) {
#pragma unroll
        for (int g = 0; g < 2; ++g) {
          const int rb = 8 * (d & 1) + 4 * g;
          sum[g] = f32x4{acc2[d >> 1][rb], acc2[d >> 1][rb + 1], acc2[d >> 1][rb + 2], acc2[d >> 1][rb + 3]};
        }
      }
#pragma unroll
    for (int k = 0; k < 3; ++k) {
      const unsigned ad = piece((mbl * 4 + kq) * 3 + k);
#pragma unroll
      for (int g = 0; g < 2; ++g) sum[g] += *(LAS f32x4*)(ad + g * 1024);
    }
    unsigned a0_ = cvtpk(f32x2{sum[0][0], sum[0][1]}), a1_ = cvtpk(f32x2{sum[0][2], sum[0][3]});
    unsigned b0_ = cvtpk(f32x2{sum[1][0], sum[1][1]}), b1_ = cvtpk(f32x2{sum[1][2], sum[1][3]});
    auto t0 = __builtin_amdgcn_permlane32_swap(a0_, b0_, false, false);
    auto t1 = __builtin_amdgcn_permlane32_swap(a1_, b1_, false, false);
    v4u o = {t0[0], t1[0], t0[1], t1[1]};
    const unsigned row = 64 * mt2 + 32 * mbl + (lane & 31);
    const unsigned col = 64 * nt2 + 16 * kq + 8 * hh;
    *(GAS v4u*)((GAS unsigned short*)po + row * D_ + col) = o;
  }
  if (b == 0 && w == 4) {
    for (unsigned spin = 0; spin < (1u << 20); ++spin) {
      const bool ok = lane >= 48 || (done.x == tagx && done.y == tagy);
      if (__all(ok)) break;
      __builtin_amdgcn_s_sleep(1);
      done = __builtin_amdgcn_raw_buffer_load_b64(rsWS, lane * 8, WS_DONE, AUX_SC1);
    }
    if (lane == 0) { v2u tg = {tagx, tagy}; __builtin_amdgcn_raw_buffer_store_b64(tg, rsWS, 0, WS_EPOCH, AUX_SC1); }
  }
  STAMP(7);
  asm volatile(".Lffn_pf_end:");
}

extern "C" void kernel_launch(void* const* d_in, const int* in_sizes, int n_in, void* d_out, int out_size, void* d_ws, size_t ws_size,
                              hipStream_t stream) {
  (void)in_sizes; (void)n_in; (void)out_size; (void)ws_size;
  (void)hipFuncSetAttribute((const void*)ffn_fused, hipFuncAttributeMaxDynamicSharedMemorySize, 160 * 1024);
  ffn_fused<<<256, 512, 160 * 1024, stream>>>((const unsigned short*)d_in[0], (const unsigned short*)d_in[1], (const unsigned short*)d_in[2],
                                              (const unsigned short*)d_in[3], (unsigned short*)d_out, (unsigned char*)d_ws
                                              );
}
```

```cpp
#include <hip/hip_runtime.h>
#include <hip/hip_bf16.h>

typedef short bf16x8 __attribute__((ext_vector_type(8)));
typedef short s16x4 __attribute__((ext_vector_type(4)));
typedef float f32x4 __attribute__((ext_vector_type(4)));
typedef float f32x16 __attribute__((ext_vector_type(16)));
typedef float f32x2 __attribute__((ext_vector_type(2)));
typedef __bf16 bf16x2_t __attribute__((ext_vector_type(2)));
typedef unsigned v4u __attribute__((ext_vector_type(4)));
typedef unsigned v2u __attribute__((ext_vector_type(2)));
#define LAS __attribute__((address_space(3)))
#define GAS __attribute__((address_space(1)))
#define DI __device__ __forceinline__

constexpr int M_ = 2048, D_ = 512, F_ = 1376;
constexpr int WROW = F_ * 2;
constexpr int SLOT = 40960, XOFF = 16384, BOFF = 32768;
constexpr int NKS = F_ / 16;
constexpr int WS_EPOCH = 0;
constexpr int WS_DONE = 1024;
constexpr int WS_FLAGS = 4096;
constexpr int WS_H = 65536;
constexpr int WS_BYTES = WS_H + 64 * NKS * 1024;
constexpr int AUX_SC1 = 16;
#define HPF 7
#define DK1 3
#define PSLEEP 4
#define CODEPF 1

DI unsigned rfl(unsigned x) { return __builtin_amdgcn_readfirstlane(x); }
DI void dma16(unsigned m0v, unsigned voff, __amdgpu_buffer_rsrc_t rs, unsigned soff) {
  asm volatile("s_mov_b32 m0, %0\n\ts_nop 0\n\tbuffer_load_dwordx4 %1, %2, %3 offen lds"
               :: "s"(m0v), "v"(voff), "s"(rs), "s"(soff) : "memory");
}
DI void pf4(unsigned m0v, unsigned voff, __amdgpu_buffer_rsrc_t rs) {
  asm volatile("s_mov_b32 m0, %0\n\ts_nop 0\n\tbuffer_load_dword %1, %2, 0 offen lds" :: "s"(m0v), "v"(voff), "s"(rs) : "memory");
}
DI unsigned cvtpk(f32x2 v) {
  bf16x2_t b = __builtin_convertvector(v, bf16x2_t);
  return __builtin_bit_cast(unsigned, b);
}
DI bf16x8 trfrag(unsigned addr0, unsigned addr1) {
  s16x4 a = __builtin_amdgcn_ds_read_tr16_b64_v4i16((LAS s16x4*)addr0);
  s16x4 b = __builtin_amdgcn_ds_read_tr16_b64_v4i16((LAS s16x4*)addr1);
  return __builtin_shufflevector(a, b, 0, 1, 2, 3, 4, 5, 6, 7);
}
DI unsigned silu_mul2(f32x2 g, f32x2 u) {
  const f32x2 t = g * -1.44269504088896340736f;
  f32x2 e = {__builtin_amdgcn_exp2f(t.x), __builtin_amdgcn_exp2f(t.y)};
  e = e + 1.0f;
  const f32x2 rc = {__builtin_amdgcn_rcpf(e.x), __builtin_amdgcn_rcpf(e.y)};
  return cvtpk((g * u) * rc);
}
#define WAIT_V(N) asm volatile("s_waitcnt vmcnt(" #N ")" ::: "memory")
#define BAR() asm volatile("s_barrier" ::: "memory")
#define LBAR() asm volatile("s_waitcnt lgkmcnt(0)\n\ts_barrier" ::: "memory")
#define VBAR(N) asm volatile("s_waitcnt vmcnt(" #N ")\n\ts_barrier" ::: "memory")
#define MFMA32(a, b, c) __builtin_amdgcn_mfma_f32_32x32x16_bf16((a), (b), (c), 0, 0, 0)
#define SGB(mask, n) __builtin_amdgcn_sched_group_barrier((mask), (n), 0)

#define STAMP(i)

struct Frags { bf16x8 x; bf16x8 wv[2][3]; };

__global__ __launch_bounds__(512, 2) void ffn_fused(const unsigned short* __restrict__ x, const unsigned short* __restrict__ wg,
                                                    const unsigned short* __restrict__ wu, const unsigned short* __restrict__ wd,
                                                    unsigned short* __restrict__ out, unsigned char* __restrict__ ws
                                                    ) {
  extern __shared__ __attribute__((aligned(16))) char lds_[];
  const unsigned ldsb = (unsigned)(size_t)lds_;
  const unsigned tid = threadIdx.x, lane = tid & 63;
  const unsigned w = rfl(tid >> 6);
  const unsigned b = blockIdx.x, xcd = b & 7, r = b >> 3;
  STAMP(0);

  const unsigned pmi = r & 7, pfi = r >> 3;
  const unsigned pm = 8 * (xcd & 1) + pmi, pf = 4 * (xcd >> 1) + pfi;
  const bool producer = pf < 15;
  const unsigned f0 = pf < 14 ? 96 * pf : 1280;
  const bool loader = w < 4;
  const unsigned kq = w & 3, mbl = w >> 2;

  unsigned long long px = (unsigned long long)x, pw = (unsigned long long)(kq < 2 ? wg : wu), pd = (unsigned long long)wd,
                     po = (unsigned long long)out, pws = (unsigned long long)ws;
  asm volatile("" : "+s"(px), "+s"(pw), "+s"(pd), "+s"(po), "+s"(pws));
  unsigned long long epoch = *(const __attribute__((address_space(4))) unsigned long long*)(pws + WS_EPOCH);

  if (loader && producer) {
    const unsigned lw = w;
    const __amdgpu_buffer_rsrc_t rsX = __builtin_amdgcn_make_buffer_rsrc((void*)px, (short)0, M_ * D_ * 2, 0x00020000);
    const __amdgpu_buffer_rsrc_t rsW = __builtin_amdgcn_make_buffer_rsrc((void*)pw, (short)0, D_ * F_ * 2, 0x00020000);
    const unsigned rot1 = (4 * (pmi & 1) + 2 * (xcd >> 1) + (xcd & 1)) & 7;
    const unsigned voffX0 = (lane >> 3) * 1024 + (((lane & 7) ^ (lane >> 4)) << 4);
    const unsigned voffX1 = voffX0 ^ 64;
    const unsigned voffW = (lane >> 2) * WROW + ((lane & 3) << 4);
    const unsigned sX = (pm * 128 + 16 * lw) * 1024;
    const unsigned sW = ((lw & 1) * 16) * WROW + f0 * 2;
    const unsigned mX = ldsb + XOFF + 16 * lw * 128;
    const unsigned mA = ldsb + (lw >> 1) * 4096 + (lw & 1) * 1024;
    const unsigned mB = ldsb + BOFF + (lw >> 1) * 2048 + (lw & 1) * 1024;
    auto dmaX = [&](unsigned xh) {
      const unsigned j = xh >> 1, h = xh & 1, sl = (j + rot1) & 7;
      const unsigned m0 = mX + (j & 3) * SLOT + h * 8192, so = sX + h * 65536 + sl * 128;
      dma16(m0, voffX0, rsX, so);
      dma16(m0 + 1024, voffX1, rsX, so + 8192);
    };
    auto dmaWA = [&](unsigned p) {
      const unsigned j = p >> 1, h = p & 1, sl = (j + rot1) & 7;
      const unsigned m0 = mA + (j & 3) * SLOT + h * 8192, so = sW + (sl * 64 + h * 32) * WROW;
      dma16(m0, voffW, rsW, so);
      dma16(m0 + 2048, voffW, rsW, so + 64);
    };
    auto dmaWB = [&](unsigned p) {
      const unsigned j = p >> 1, h = p & 1, sl = (j + rot1) & 7;
      dma16(mB + (j & 3) * SLOT + h * 4096, voffW, rsW, sW + (sl * 64 + h * 32) * WROW + 128);
    };
    dmaWA(0); dmaWB(0); dmaX(0); dmaX(1);
    dmaWA(1); dmaWB(1); dmaX(2);
    dmaWA(2); dmaWB(2); dmaX(3);
    asm volatile("" :: "s"(epoch));
    STAMP(1);
#pragma unroll 1
    for (unsigned p = 0; p < 5; ++p) {
      VBAR(10);
      dmaWA(p + 3); dmaWB(p + 3); dmaX(p + 4);
    }
#pragma unroll 1
    for (unsigned p = 5; p < 12; ++p) {
      VBAR(8);
      dmaWA(p + 3); dmaX(p + 4);
    }
    VBAR(8);
    dmaWA(15);
    VBAR(6);
    dmaWB(8); dmaWB(9); dmaWB(10); dmaWB(11);
    VBAR(6);
    dmaWB(12); dmaWB(13); dmaWB(14); dmaWB(15);
    VBAR(8);
    BAR();
    STAMP(2);
  }

  const __amdgpu_buffer_rsrc_t rsD = __builtin_amdgcn_make_buffer_rsrc((void*)pd, (short)0, F_ * D_ * 2, 0x00020000);
  const __amdgpu_buffer_rsrc_t rsWS = __builtin_amdgcn_make_buffer_rsrc((void*)pws, (short)0, WS_BYTES, 0x00020000);
  const unsigned mt2 = 4 * xcd + (r >> 3), nt2 = r & 7;
  const unsigned nk = kq < 2 ? 22 : 21;
  const unsigned e2b = kq * 8 - (kq >> 1 & kq & 1);
  const unsigned voffD = (lane >> 3) * 1024 + (((lane & 7) ^ (((lane >> 4) & 1) << 2)) << 4);
  auto dmaDr = [&](int i0, int i1) {
#pragma unroll
    for (int part = 0; part < 3; ++part) {
      const int lo = i0 > 7 * part ? i0 : 7 * part, hi = (part < 2 && i1 > 7 * part + 7) ? 7 * part + 7 : i1;
      if (lo < hi) {
        const unsigned e = (part < 2 ? 7 * kq : e2b) + (lo - 7 * part);
        unsigned m0 = ldsb + kq * SLOT + (lo >= 20 ? lo - 20 : lo) * 2048;
        unsigned so = (6 * (e >> 1) + (e & 1) + 2 * part) * 16384 + nt2 * 128;
        unsigned st = 16384 + (e & 1) * 65536;
#pragma unroll 1
        for (int i = lo; i < hi; ++i) {
          const unsigned sq = (part == 2 && so >= 88 * 16384) ? so - 65536 : so;
          dma16(m0, voffD, rsD, sq);
          dma16(m0 + 1024, voffD, rsD, sq + 8192);
          m0 += 2048; so += st; st ^= 65536;
        }
      }
    }
  };

  asm volatile("" : "+s"(epoch));
  const unsigned long long tag64 = epoch + 1ull;
  const unsigned tagx = (unsigned)tag64, tagy = (unsigned)(tag64 >> 32);

  if (loader) {
    dmaDr(0, DK1);
    STAMP(8);
    if (producer) asm volatile("s_waitcnt vmcnt(%0)\n\ts_barrier" :: "n"(2 * DK1) : "memory");
    STAMP(9);
    dmaDr(DK1, 16);
    STAMP(10);
    VBAR(16);
    STAMP(3);
    dmaDr(16, 20);
    STAMP(4);
  } else {
    if (w == 4 && producer) {
      unsigned long long pc;
      unsigned len;
      asm volatile("s_getpc_b64 %0\n.Lffn_pf_here:\n\ts_mov_b32 %1, .Lffn_pf_end-.Lffn_pf_here" : "=s"(pc), "=s"(len));
      pf4(ldsb + 3 * SLOT, lane < 4 ? r * 512 + lane * 128 : 0x40000000u, __builtin_amdgcn_make_buffer_rsrc((void*)pc, (short)0, (int)len, 0x00020000));
    }
    const v2u tg = {tagx, tagy};
    if (producer) {
      const unsigned mbw = w & 3;
      const unsigned r32 = lane & 31, h2 = lane >> 5;
      unsigned xr[4];
#pragma unroll
      for (int c2 = 0; c2 < 4; ++c2) xr[c2] = ldsb + XOFF + (32 * mbw + r32) * 128 + (((2 * c2 + h2) ^ ((r32 >> 1) & 7)) << 4);
      const unsigned wr = ldsb + (8 * h2 + ((lane & 15) >> 2)) * 64 + ((lane >> 4) & 1) * 32 + 8 * (lane & 3);

      f32x16 acc[2][3];
#pragma unroll
      for (int a = 0; a < 2; ++a)
#pragma unroll
        for (int j = 0; j < 3; ++j) {
#pragma unroll
          for (int i = 0; i < 16; ++i) acc[a][j][i] = 0.f;
          asm volatile("" : "+v"(acc[a][j]));
        }
      auto rd = [&](Frags& Fr, unsigned slot, int h, int k, int mode) {
        Fr.x = *(LAS bf16x8*)(xr[2 * h + k] + slot);
#pragma unroll
        for (int a = 0; a < 2; ++a) {
          if (mode & 1) {
#pragma unroll
            for (int j = 0; j < 2; ++j) {
              const unsigned ad = wr + slot + h * 8192 + a * 4096 + j * 2048 + k * 1024;
              Fr.wv[a][j] = trfrag(ad, ad + 256);
            }
          }
          if (mode & 2) {
            const unsigned ad = wr + slot + BOFF + h * 4096 + a * 2048 + k * 1024;
            Fr.wv[a][2] = trfrag(ad, ad + 256);
          }
        }
      };
      auto mm = [&](const Frags& Fr, int mode) {
#pragma unroll
        for (int a = 0; a < 2; ++a)
#pragma unroll
          for (int j = 0; j < 3; ++j)
            if (j < 2 ? (mode & 1) : (mode & 2)) acc[a][j] = MFMA32(Fr.wv[a][j], Fr.x, acc[a][j]);
      };
      auto il = [&](int nm) {
        SGB(0x008, 1); SGB(0x100, 3);
#pragma unroll
        for (int i = 1; i < nm; ++i) { SGB(0x008, 1); SGB(0x100, 2); }
        __builtin_amdgcn_sched_barrier(0);
      };
      const unsigned hoff = lane * 16 + WS_H + ((4 * pm + mbw) * NKS + 2 * pf) * 1024;
      auto epi = [&](int j) {
#pragma unroll
        for (int sx = 0; sx < 2; ++sx) {
          v4u o;
#pragma unroll
          for (int e = 0; e < 4; ++e) {
            const int i0 = 8 * sx + 2 * e;
            o[e] = silu_mul2(f32x2{acc[0][j][i0], acc[0][j][i0 + 1]}, f32x2{acc[1][j][i0], acc[1][j][i0 + 1]});
          }
          __builtin_amdgcn_raw_buffer_store_b128(o, rsWS, hoff + (28 * j + sx) * 1024, 0, AUX_SC1);
        }
      };
      Frags A, B;
      __builtin_amdgcn_s_setprio(1);
      BAR();
      STAMP(1);
      rd(A, 0, 0, 0, 3);
#pragma unroll 1
      for (unsigned j = 0; j < 4; ++j) {
        const unsigned slot = j * SLOT, slotn = ((j + 1) & 3) * SLOT;
        rd(B, slot, 0, 1, 3); mm(A, 3); il(6);
        if (j == 1) WAIT_V(0);
        BAR();
        rd(A, slot, 1, 0, 3); mm(B, 3); il(6);
        rd(B, slot, 1, 1, 3); mm(A, 3); il(6);
        BAR();
        rd(A, slotn, 0, 0, 3);
        mm(B, 3); il(6);
      }
      bf16x8 xk[16];
#pragma unroll
      for (int j = 0; j < 4; ++j) {
        const unsigned slot = j * SLOT;
        xk[4 * j] = A.x;
        rd(B, slot, 0, 1, 1); mm(A, 1); il(4);
        BAR();
        xk[4 * j + 1] = B.x;
        rd(A, slot, 1, 0, 1); mm(B, 1); il(4);
        xk[4 * j + 2] = A.x;
        rd(B, slot, 1, 1, 1); mm(A, 1); il(4);
        if (j == 3) asm volatile("s_waitcnt lgkmcnt(0)" ::: "memory");
        BAR();
        xk[4 * j + 3] = B.x;
        if (j < 3) { rd(A, slot + SLOT, 0, 0, 1); mm(B, 1); il(4); }
        else mm(B, 1);
      }
      STAMP(2);
      if (pf < 14) { epi(0); epi(1); }
      STAMP(8);
      BAR();
      STAMP(9);
      {
        auto rdb = [&](bf16x8 (&wb)[2], int t) {
#pragma unroll
          for (int a = 0; a < 2; ++a) {
            const unsigned ad = wr + (t >> 2) * SLOT + BOFF + ((t >> 1) & 1) * 4096 + a * 2048 + (t & 1) * 1024;
            wb[a] = trfrag(ad, ad + 256);
          }
        };
        bf16x8 wb[3][2];
        rdb(wb[0], 0); rdb(wb[1], 1);
#pragma unroll
        for (int t = 0; t < 16; ++t) {
          if (t + 2 < 16) rdb(wb[(t + 2) % 3], t + 2);
          acc[0][2] = MFMA32(wb[t % 3][0], xk[t], acc[0][2]);
          acc[1][2] = MFMA32(wb[t % 3][1], xk[t], acc[1][2]);
          SGB(0x008, 1); SGB(0x100, 2); SGB(0x008, 1); SGB(0x100, 2);
          __builtin_amdgcn_sched_barrier(0);
        }
      }
      STAMP(10);
      const unsigned fo = WS_FLAGS + ((4 * pm + mbw) * 32 + pf) * 8;
      WAIT_V(0);
      if (lane == 0 && pf < 14) __builtin_amdgcn_raw_buffer_store_b64(tg, rsWS, 0, fo, AUX_SC1);
      STAMP(11);
      epi(2);
      __builtin_amdgcn_s_setprio(0);
      STAMP(12);
      WAIT_V(0);
      if (lane == 0) __builtin_amdgcn_raw_buffer_store_b64(tg, rsWS, 0, fo + 128, AUX_SC1);
      STAMP(3);
      LBAR();
    } else {
      BAR();
      if (w == 4 && lane == 0) __builtin_amdgcn_raw_buffer_store_b64(tg, rsWS, 0, WS_DONE + (32 + (xcd & 1) * 8 + pmi) * 8, AUX_SC1);
    }
    STAMP(4);
  }

  const unsigned hh = lane >> 5, blk = (lane >> 4) & 1, q2 = (lane & 15) >> 2, p2 = lane & 3;
  const unsigned dr0 = ldsb + kq * SLOT + (4 * hh + q2) * 128 + ((q2 >> 1) * 64) + blk * 32 + 8 * p2;
  const unsigned dr1 = dr0 ^ 64;
  f32x16 acc2[2];
#pragma unroll
  for (int i = 0; i < 16; ++i) { acc2[0][i] = 0.f; acc2[1][i] = 0.f; }
  asm volatile("" : "+v"(acc2[0]), "+v"(acc2[1]));
  {
    const unsigned mb2 = 2 * mt2 + mbl;
    const unsigned hb = WS_H + (mb2 * NKS) * 1024;
    const unsigned foff = WS_FLAGS + (mb2 * 32 + (lane & 31)) * 8;
    const unsigned loA = (7 * kq) >> 1, hiA = (7 * kq + 6) >> 1, loB = e2b >> 1, hiB = (e2b + nk - 15) >> 1;
    const unsigned needA = ((2u << hiA) - 1u) & ~((1u << loA) - 1u);
    const unsigned needB = (((2u << hiB) - 1u) & ~((1u << loB) - 1u)) << 16;
    unsigned seen = 0;
    auto wait_flags = [&](unsigned need) {
      for (unsigned spin = 0; (seen & need) != need && spin < (1u << 20); ++spin) {
        asm volatile("" ::: "memory");
        const v2u f = __builtin_amdgcn_raw_buffer_load_b64(rsWS, foff, 0, AUX_SC1);
        seen = (unsigned)__builtin_amdgcn_ballot_w64(f.x == tagx && f.y == tagy);
        if ((seen & need) != need) __builtin_amdgcn_s_sleep(PSLEEP);
      }
      __builtin_amdgcn_fence(__ATOMIC_ACQUIRE, "wavefront");
    };
    v4u hf[HPF];
    auto ldh = [&](int i) {
      if (i == 0) wait_flags(needA);
      if (i == 14) wait_flags(needB);
      const unsigned kap = i < 14 ? 7 * kq + (i < 7 ? i : i + 21) : i < 21 ? 56 + e2b + (i - 14) : 56 + e2b + nk - 15;
      return __builtin_amdgcn_raw_buffer_load_b128(rsWS, lane * 16, hb + kap * 1024, AUX_SC1);
    };
    bf16x8 dn0 = trfrag(dr0, dr0 + 1024), dn1 = trfrag(dr1, dr1 + 1024);
#pragma unroll
    for (int i = 0; i < HPF; ++i) { hf[i] = ldh(i); if (i == 0) { STAMP(5); } }
#pragma unroll
    for (int i = 0; i < 22; ++i) {
      __builtin_amdgcn_sched_barrier(0);
      if (i == 7) {
        LBAR();
        if (loader) { dmaDr(20, 21); if (nk == 22) dmaDr(21, 22); }
      }
      if (i == 7 + HPF + 1) LBAR();
      const bf16x8 d0 = dn0, d1 = dn1;
      if (i + 1 < 22) {
        const unsigned o = (i + 1 >= 20 ? i + 1 - 20 : i + 1) * 2048;
        dn0 = trfrag(dr0 + o, dr0 + o + 1024); dn1 = trfrag(dr1 + o, dr1 + o + 1024);
      }
      v4u hraw = hf[i % HPF];
      if (i == 21) { const unsigned msk = nk == 22 ? 0xffffffffu : 0u; hraw &= msk; }
      const bf16x8 hv = __builtin_bit_cast(bf16x8, hraw);
      acc2[0] = MFMA32(d0, hv, acc2[0]);
      acc2[1] = MFMA32(d1, hv, acc2[1]);
      if (i + HPF < 22) hf[i % HPF] = ldh(i + HPF);
      if (i == 7 + HPF + 1 && nt2 == 0 && w == 4 && lane == 0) {
        const v2u tg = {tagx, tagy};
        __builtin_amdgcn_raw_buffer_store_b64(tg, rsWS, 0, WS_DONE + mt2 * 8, AUX_SC1);
      }
    }
    __builtin_amdgcn_sched_barrier(0);
  }
  STAMP(6);
  {
    const unsigned wa = ldsb + 4096 + (mbl * 4 + kq) * 2048 + lane * 16;
#pragma unroll
    for (int d = 0; d < 4; ++d)
#pragma unroll
      for (int g = 0; g < 2; ++g) {
        const int rb = 8 * (d & 1) + 4 * g;
        *(LAS f32x4*)(wa + d * SLOT + g * 1024) = f32x4{acc2[d >> 1][rb], acc2[d >> 1][rb + 1], acc2[d >> 1][rb + 2], acc2[d >> 1][rb + 3]};
      }
    const unsigned row = 64 * mt2 + 32 * mbl + (lane & 31);
    const unsigned col = 64 * nt2 + 16 * kq + 8 * hh;
    GAS v4u* op = (GAS v4u*)((GAS unsigned short*)po + row * D_ + col);
    LBAR();
    const unsigned ra = ldsb + kq * SLOT + 4096 + mbl * 8192 + lane * 16;
    f32x4 pv[4][2];
#pragma unroll
    for (int s = 0; s < 4; ++s)
#pragma unroll
      for (int g = 0; g < 2; ++g) pv[s][g] = *(LAS f32x4*)(ra + s * 2048 + g * 1024);
    const f32x4 s0 = (pv[0][0] + pv[1][0]) + (pv[2][0] + pv[3][0]), s1 = (pv[0][1] + pv[1][1]) + (pv[2][1] + pv[3][1]);
    const unsigned a0_ = cvtpk(f32x2{s0[0], s0[1]}), a1_ = cvtpk(f32x2{s0[2], s0[3]});
    const unsigned b0_ = cvtpk(f32x2{s1[0], s1[1]}), b1_ = cvtpk(f32x2{s1[2], s1[3]});
    auto t0 = __builtin_amdgcn_permlane32_swap(a0_, b0_, false, false);
    auto t1 = __builtin_amdgcn_permlane32_swap(a1_, b1_, false, false);
    *op = v4u{t0[0], t1[0], t0[1], t1[1]};
  }
  if (b == 0 && w == 4) {
    for (unsigned spin = 0; spin < (1u << 20); ++spin) {
      asm volatile("" ::: "memory");
      const v2u done = __builtin_amdgcn_raw_buffer_load_b64(rsWS, lane * 8, WS_DONE, AUX_SC1);
      if (__all(lane >= 48 || (done.x == tagx && done.y == tagy))) break;
    }
    if (lane == 0) { v2u tg = {tagx, tagy}; __builtin_amdgcn_raw_buffer_store_b64(tg, rsWS, 0, WS_EPOCH, AUX_SC1); }
  }
  STAMP(7);
  asm volatile(".Lffn_pf_end:");
}

extern "C" void kernel_launch(void* const* d_in, const int* in_sizes, int n_in, void* d_out, int out_size, void* d_ws, size_t ws_size,
                              hipStream_t stream) {
  (void)in_sizes; (void)n_in; (void)out_size; (void)ws_size;
  (void)hipFuncSetAttribute((const void*)ffn_fused, hipFuncAttributeMaxDynamicSharedMemorySize, 160 * 1024);
  ffn_fused<<<256, 512, 160 * 1024, stream>>>((const unsigned short*)d_in[0], (const unsigned short*)d_in[1], (const unsigned short*)d_in[2],
                                              (const unsigned short*)d_in[3], (unsigned short*)d_out, (unsigned char*)d_ws
                                              );
}
```

```cpp
#include <hip/hip_runtime.h>
#include <hip/hip_bf16.h>

typedef short bf16x8 __attribute__((ext_vector_type(8)));
typedef short s16x4 __attribute__((ext_vector_type(4)));
typedef float f32x4 __attribute__((ext_vector_type(4)));
typedef float f32x16 __attribute__((ext_vector_type(16)));
typedef float f32x2 __attribute__((ext_vector_type(2)));
typedef __bf16 bf16x2_t __attribute__((ext_vector_type(2)));
typedef unsigned v4u __attribute__((ext_vector_type(4)));
typedef unsigned v2u __attribute__((ext_vector_type(2)));
#define LAS __attribute__((address_space(3)))
#define GAS __attribute__((address_space(1)))
#define DI __device__ __forceinline__

constexpr int M_ = 2048, D_ = 512, F_ = 1376;
constexpr int WROW = F_ * 2;
constexpr int SLOT = 40960, XOFF = 16384, BOFF = 32768;
constexpr int NKS = F_ / 16;
constexpr int WS_EPOCH = 0;
constexpr int WS_DONE = 1024;
constexpr int WS_FLAGS = 4096;
constexpr int WS_H = 65536;
constexpr int WS_BYTES = WS_H + 64 * NKS * 1024;
constexpr int AUX_SC1 = 16;
#define HPF 7
#define DK1 3
#define PSLEEP 4
#define CODEPF 1

DI unsigned rfl(unsigned x) { return __builtin_amdgcn_readfirstlane(x); }
DI void dma16(unsigned m0v, unsigned voff, __amdgpu_buffer_rsrc_t rs, unsigned soff) {
  asm volatile("s_mov_b32 m0, %0\n\ts_nop 0\n\tbuffer_load_dwordx4 %1, %2, %3 offen lds"
               :: "s"(m0v), "v"(voff), "s"(rs), "s"(soff) : "memory");
}
DI void pf4(unsigned m0v, unsigned voff, __amdgpu_buffer_rsrc_t rs) {
  asm volatile("s_mov_b32 m0, %0\n\ts_nop 0\n\tbuffer_load_dword %1, %2, 0 offen lds" :: "s"(m0v), "v"(voff), "s"(rs) : "memory");
}
DI unsigned cvtpk(f32x2 v) {
  bf16x2_t b = __builtin_convertvector(v, bf16x2_t);
  return __builtin_bit_cast(unsigned, b);
}
DI bf16x8 trfrag(unsigned addr0, unsigned addr1) {
  s16x4 a = __builtin_amdgcn_ds_read_tr16_b64_v4i16((LAS s16x4*)addr0);
  s16x4 b = __builtin_amdgcn_ds_read_tr16_b64_v4i16((LAS s16x4*)addr1);
  return __builtin_shufflevector(a, b, 0, 1, 2, 3, 4, 5, 6, 7);
}
DI unsigned silu_mul2(f32x2 g, f32x2 u) {
  const f32x2 t = g * -1.44269504088896340736f;
  f32x2 e = {__builtin_amdgcn_exp2f(t.x), __builtin_amdgcn_exp2f(t.y)};
  e = e + 1.0f;
  const f32x2 rc = {__builtin_amdgcn_rcpf(e.x), __builtin_amdgcn_rcpf(e.y)};
  return cvtpk((g * u) * rc);
}
#define WAIT_V(N) asm volatile("s_waitcnt vmcnt(" #N ")" ::: "memory")
#define BAR() asm volatile("s_barrier" ::: "memory")
#define LBAR() asm volatile("s_waitcnt lgkmcnt(0)\n\ts_barrier" ::: "memory")
#define VBAR(N) asm volatile("s_waitcnt vmcnt(" #N ")\n\ts_barrier" ::: "memory")
#define MFMA32(a, b, c) __builtin_amdgcn_mfma_f32_32x32x16_bf16((a), (b), (c), 0, 0, 0)
#define SGB(mask, n) __builtin_amdgcn_sched_group_barrier((mask), (n), 0)

#define STAMP(i)

struct Frags { bf16x8 x; bf16x8 wv[2][3]; };

__global__ __launch_bounds__(512, 2) void ffn_fused(const unsigned short* __restrict__ x, const unsigned short* __restrict__ wg,
                                                    const unsigned short* __restrict__ wu, const unsigned short* __restrict__ wd,
                                                    unsigned short* __restrict__ out, unsigned char* __restrict__ ws
                                                    ) {
  extern __shared__ __attribute__((aligned(16))) char lds_[];
  const unsigned ldsb = (unsigned)(size_t)lds_;
  const unsigned tid = threadIdx.x, lane = tid & 63;
  const unsigned w = rfl(tid >> 6);
  const unsigned b = blockIdx.x, xcd = b & 7, r = b >> 3;
  STAMP(0);

  const unsigned pmi = r & 7, pfi = r >> 3;
  const unsigned pm = 8 * (xcd & 1) + pmi, pf = 4 * (xcd >> 1) + pfi;
  const bool producer = pf < 15;
  const unsigned f0 = pf < 14 ? 96 * pf : 1280;
  const bool loader = w < 4;
  const unsigned kq = w & 3, mbl = w >> 2;

  unsigned long long px = (unsigned long long)x, pw = (unsigned long long)(kq < 2 ? wg : wu), pd = (unsigned long long)wd,
                     po = (unsigned long long)out, pws = (unsigned long long)ws;
  asm volatile("" : "+s"(px), "+s"(pw), "+s"(pd), "+s"(po), "+s"(pws));
  unsigned long long epoch = *(const __attribute__((address_space(4))) unsigned long long*)(pws + WS_EPOCH);

  if (loader && producer) {
    const unsigned lw = w;
    const __amdgpu_buffer_rsrc_t rsX = __builtin_amdgcn_make_buffer_rsrc((void*)px, (short)0, M_ * D_ * 2, 0x00020000);
    const __amdgpu_buffer_rsrc_t rsW = __builtin_amdgcn_make_buffer_rsrc((void*)pw, (short)0, D_ * F_ * 2, 0x00020000);
    const unsigned rot1 = (4 * (pmi & 1) + 2 * (xcd >> 1) + (xcd & 1)) & 7;
    const unsigned voffX0 = (lane >> 3) * 1024 + (((lane & 7) ^ (lane >> 4)) << 4);
    const unsigned voffX1 = voffX0 ^ 64;
    const unsigned voffW = (lane >> 2) * WROW + ((lane & 3) << 4);
    const unsigned sX = (pm * 128 + 16 * lw) * 1024;
    const unsigned sW = ((lw & 1) * 16) * WROW + f0 * 2;
    const unsigned mX = ldsb + XOFF + 16 * lw * 128;
    const unsigned mA = ldsb + (lw >> 1) * 4096 + (lw & 1) * 1024;
    const unsigned mB = ldsb + BOFF + (lw >> 1) * 2048 + (lw & 1) * 1024;
    auto dmaX = [&](unsigned xh) {
      const unsigned j = xh >> 1, h = xh & 1, sl = (j + rot1) & 7;
      const unsigned m0 = mX + (j & 3) * SLOT + h * 8192, so = sX + h * 65536 + sl * 128;
      dma16(m0, voffX0, rsX, so);
      dma16(m0 + 1024, voffX1, rsX, so + 8192);
    };
    auto dmaWA = [&](unsigned p) {
      const unsigned j = p >> 1, h = p & 1, sl = (j + rot1) & 7;
      const unsigned m0 = mA + (j & 3) * SLOT + h * 8192, so = sW + (sl * 64 + h * 32) * WROW;
      dma16(m0, voffW, rsW, so);
      dma16(m0 + 2048, voffW, rsW, so + 64);
    };
    auto dmaWB = [&](unsigned p) {
      const unsigned j = p >> 1, h = p & 1, sl = (j + rot1) & 7;
      dma16(mB + (j & 3) * SLOT + h * 4096, voffW, rsW, sW + (sl * 64 + h * 32) * WROW + 128);
    };
    dmaWA(0); dmaWB(0); dmaX(0); dmaX(1);
    dmaWA(1); dmaWB(1); dmaX(2);
    dmaWA(2); dmaWB(2); dmaX(3);
    asm volatile("" :: "s"(epoch));
    STAMP(1);
#pragma unroll 1
    for (unsigned p = 0; p < 5; ++p) {
      VBAR(10);
      dmaWA(p + 3); dmaWB(p + 3); dmaX(p + 4);
    }
#pragma unroll 1
    for (unsigned p = 5; p < 12; ++p) {
      VBAR(8);
      dmaWA(p + 3); dmaX(p + 4);
    }
    VBAR(8);
    dmaWA(15);
    VBAR(6);
    dmaWB(8); dmaWB(9); dmaWB(10); dmaWB(11);
    VBAR(6);
    dmaWB(12); dmaWB(13); dmaWB(14); dmaWB(15);
    VBAR(8);
    BAR();
    STAMP(2);
  }

  const __amdgpu_buffer_rsrc_t rsD = __builtin_amdgcn_make_buffer_rsrc((void*)pd, (short)0, F_ * D_ * 2, 0x00020000);
  const __amdgpu_buffer_rsrc_t rsWS = __builtin_amdgcn_make_buffer_rsrc((void*)pws, (short)0, WS_BYTES, 0x00020000);
  const unsigned mt2 = 4 * xcd + (r >> 3), nt2 = r & 7;
  const unsigned nk = kq < 2 ? 22 : 21;
  const unsigned e2b = kq * 8 - (kq >> 1 & kq & 1);
  const unsigned voffD = (lane >> 3) * 1024 + (((lane & 7) ^ (((lane >> 4) & 1) << 2)) << 4);
  auto dmaDr = [&](int i0, int i1) {
#pragma unroll
    for (int part = 0; part < 3; ++part) {
      const int lo = i0 > 7 * part ? i0 : 7 * part, hi = (part < 2 && i1 > 7 * part + 7) ? 7 * part + 7 : i1;
      if (lo < hi) {
        const unsigned e = (part < 2 ? 7 * kq : e2b) + (lo - 7 * part);
        unsigned m0 = ldsb + kq * SLOT + (lo >= 20 ? lo - 20 : lo) * 2048;
        unsigned so = (6 * (e >> 1) + (e & 1) + 2 * part) * 16384 + nt2 * 128;
        unsigned st = 16384 + (e & 1) * 65536;
#pragma unroll 1
        for (int i = lo; i < hi; ++i) {
          const unsigned sq = (part == 2 && so >= 88 * 16384) ? so - 65536 : so;
          dma16(m0, voffD, rsD, sq);
          dma16(m0 + 1024, voffD, rsD, sq + 8192);
          m0 += 2048; so += st; st ^= 65536;
        }
      }
    }
  };

  asm volatile("" : "+s"(epoch));
  const unsigned long long tag64 = epoch + 1ull;
  const unsigned tagx = (unsigned)tag64, tagy = (unsigned)(tag64 >> 32);

  if (loader) {
    dmaDr(0, DK1);
    STAMP(8);
    if (producer) asm volatile("s_waitcnt vmcnt(%0)\n\ts_barrier" :: "n"(2 * DK1) : "memory");
    STAMP(9);
    dmaDr(DK1, 16);
    STAMP(10);
    VBAR(16);
    STAMP(3);
    dmaDr(16, 20);
    STAMP(4);
  } else {
    if (w == 4 && producer) {
      unsigned long long pc;
      unsigned len;
      asm volatile("s_getpc_b64 %0\n.Lffn_pf_here:\n\ts_mov_b32 %1, .Lffn_pf_end-.Lffn_pf_here" : "=s"(pc), "=s"(len));
      pf4(ldsb + 3 * SLOT, lane < 4 ? r * 512 + lane * 128 : 0x40000000u, __builtin_amdgcn_make_buffer_rsrc((void*)pc, (short)0, (int)len, 0x00020000));
    }
    const v2u tg = {tagx, tagy};
    if (producer) {
      const unsigned mbw = w & 3;
      const unsigned r32 = lane & 31, h2 = lane >> 5;
      unsigned xr[4];
#pragma unroll
      for (int c2 = 0; c2 < 4; ++c2) xr[c2] = ldsb + XOFF + (32 * mbw + r32) * 128 + (((2 * c2 + h2) ^ ((r32 >> 1) & 7)) << 4);
      const unsigned wr = ldsb + (8 * h2 + ((lane & 15) >> 2)) * 64 + ((lane >> 4) & 1) * 32 + 8 * (lane & 3);

      f32x16 acc[2][3];
#pragma unroll
      for (int a = 0; a < 2; ++a)
#pragma unroll
        for (int j = 0; j < 3; ++j) {
#pragma unroll
          for (int i = 0; i < 16; ++i) acc[a][j][i] = 0.f;
          asm volatile("" : "+v"(acc[a][j]));
        }
      auto rd = [&](Frags& Fr, unsigned slot, int h, int k, int mode) {
        Fr.x = *(LAS bf16x8*)(xr[2 * h + k] + slot);
#pragma unroll
        for (int a = 0; a < 2; ++a) {
          if (mode & 1) {
#pragma unroll
            for (int j = 0; j < 2; ++j) {
              const unsigned ad = wr + slot + h * 8192 + a * 4096 + j * 2048 + k * 1024;
              Fr.wv[a][j] = trfrag(ad, ad + 256);
            }
          }
          if (mode & 2) {
            const unsigned ad = wr + slot + BOFF + h * 4096 + a * 2048 + k * 1024;
            Fr.wv[a][2] = trfrag(ad, ad + 256);
          }
        }
      };
      auto mm = [&](const Frags& Fr, int mode) {
#pragma unroll
        for (int a = 0; a < 2; ++a)
#pragma unroll
          for (int j = 0; j < 3; ++j)
            if (j < 2 ? (mode & 1) : (mode & 2)) acc[a][j] = MFMA32(Fr.wv[a][j], Fr.x, acc[a][j]);
      };
      auto il = [&](int nm) {
        SGB(0x008, 1); SGB(0x100, 3);
#pragma unroll
        for (int i = 1; i < nm; ++i) { SGB(0x008, 1); SGB(0x100, 2); }
        __builtin_amdgcn_sched_barrier(0);
      };
      const unsigned hoff = lane * 16 + WS_H + ((4 * pm + mbw) * NKS + 2 * pf) * 1024;
      auto epi = [&](int j) {
#pragma unroll
        for (int sx = 0; sx < 2; ++sx) {
          v4u o;
#pragma unroll
          for (int e = 0; e < 4; ++e) {
            const int i0 = 8 * sx + 2 * e;
            o[e] = silu_mul2(f32x2{acc[0][j][i0], acc[0][j][i0 + 1]}, f32x2{acc[1][j][i0], acc[1][j][i0 + 1]});
          }
          __builtin_amdgcn_raw_buffer_store_b128(o, rsWS, hoff + (28 * j + sx) * 1024, 0, AUX_SC1);
        }
      };
      Frags A, B;
      __builtin_amdgcn_s_setprio(1);
      BAR();
      STAMP(1);
      rd(A, 0, 0, 0, 3);
#pragma unroll 1
      for (unsigned j = 0; j < 4; ++j) {
        const unsigned slot = j * SLOT, slotn = ((j + 1) & 3) * SLOT;
        rd(B, slot, 0, 1, 3); mm(A, 3); il(6);
        if (j == 1) WAIT_V(0);
        BAR();
        rd(A, slot, 1, 0, 3); mm(B, 3); il(6);
        rd(B, slot, 1, 1, 3); mm(A, 3); il(6);
        BAR();
        rd(A, slotn, 0, 0, 3);
        mm(B, 3); il(6);
      }
      bf16x8 xk[16];
#pragma unroll
      for (int j = 0; j < 4; ++j) {
        const unsigned slot = j * SLOT;
        xk[4 * j] = A.x;
        rd(B, slot, 0, 1, 1); mm(A, 1); il(4);
        BAR();
        xk[4 * j + 1] = B.x;
        rd(A, slot, 1, 0, 1); mm(B, 1); il(4);
        xk[4 * j + 2] = A.x;
        rd(B, slot, 1, 1, 1); mm(A, 1); il(4);
        if (j == 3) asm volatile("s_waitcnt lgkmcnt(0)" ::: "memory");
        BAR();
        xk[4 * j + 3] = B.x;
        if (j < 3) { rd(A, slot + SLOT, 0, 0, 1); mm(B, 1); il(4); }
        else mm(B, 1);
      }
      STAMP(2);
      if (pf < 14) { epi(0); epi(1); }
      STAMP(8);
      BAR();
      STAMP(9);
      {
        auto rdb = [&](bf16x8 (&wb)[2], int t) {
#pragma unroll
          for (int a = 0; a < 2; ++a) {
            const unsigned ad = wr + (t >> 2) * SLOT + BOFF + ((t >> 1) & 1) * 4096 + a * 2048 + (t & 1) * 1024;
            wb[a] = trfrag(ad, ad + 256);
          }
        };
        bf16x8 wb[3][2];
        rdb(wb[0], 0); rdb(wb[1], 1);
#pragma unroll
        for (int t = 0; t < 16; ++t) {
          if (t + 2 < 16) rdb(wb[(t + 2) % 3], t + 2);
          acc[0][2] = MFMA32(wb[t % 3][0], xk[t], acc[0][2]);
          acc[1][2] = MFMA32(wb[t % 3][1], xk[t], acc[1][2]);
          SGB(0x008, 1); SGB(0x100, 2); SGB(0x008, 1); SGB(0x100, 2);
          __builtin_amdgcn_sched_barrier(0);
        }
      }
      STAMP(10);
      const unsigned fo = WS_FLAGS + ((4 * pm + mbw) * 32 + pf) * 8;
      WAIT_V(0);
      if (lane == 0 && pf < 14) __builtin_amdgcn_raw_buffer_store_b64(tg, rsWS, 0, fo, AUX_SC1);
      STAMP(11);
      epi(2);
      __builtin_amdgcn_s_setprio(0);
      STAMP(12);
      WAIT_V(0);
      if (lane == 0) __builtin_amdgcn_raw_buffer_store_b64(tg, rsWS, 0, fo + 128, AUX_SC1);
      STAMP(3);
      LBAR();
    } else {
      BAR();
      if (w == 4 && lane == 0) __builtin_amdgcn_raw_buffer_store_b64(tg, rsWS, 0, WS_DONE + (32 + (xcd & 1) * 8 + pmi) * 8, AUX_SC1);
    }
    STAMP(4);
  }

  const unsigned hh = lane >> 5, blk = (lane >> 4) & 1, q2 = (lane & 15) >> 2, p2 = lane & 3;
  const unsigned dr0 = ldsb + kq * SLOT + (4 * hh + q2) * 128 + ((q2 >> 1) * 64) + blk * 32 + 8 * p2;
  const unsigned dr1 = dr0 ^ 64;
  f32x16 acc2[2];
#pragma unroll
  for (int i = 0; i < 16; ++i) { acc2[0][i] = 0.f; acc2[1][i] = 0.f; }
  asm volatile("" : "+v"(acc2[0]), "+v"(acc2[1]));
  {
    const unsigned mb2 = 2 * mt2 + mbl;
    const unsigned hb = WS_H + (mb2 * NKS) * 1024;
    const unsigned foff = WS_FLAGS + (mb2 * 32 + (lane & 31)) * 8;
    const unsigned loA = (7 * kq) >> 1, hiA = (7 * kq + 6) >> 1, loB = e2b >> 1, hiB = (e2b + nk - 15) >> 1;
    const unsigned needA = ((2u << hiA) - 1u) & ~((1u << loA) - 1u);
    const unsigned needB = (((2u << hiB) - 1u) & ~((1u << loB) - 1u)) << 16;
    unsigned seen = 0;
    auto wait_flags = [&](unsigned need) {
      for (unsigned spin = 0; (seen & need) != need && spin < (1u << 20); ++spin) {
        asm volatile("" ::: "memory");
        const v2u f = __builtin_amdgcn_raw_buffer_load_b64(rsWS, foff, 0, AUX_SC1);
        seen = (unsigned)__builtin_amdgcn_ballot_w64(f.x == tagx && f.y == tagy);
        if ((seen & need) != need) __builtin_amdgcn_s_sleep(PSLEEP);
      }
      __builtin_amdgcn_fence(__ATOMIC_ACQUIRE, "wavefront");
    };
    v4u hf[HPF];
    auto ldh = [&](int i) {
      if (i == 0) wait_flags(needA);
      if (i == 14) wait_flags(needB);
      const unsigned kap = i < 14 ? 7 * kq + (i < 7 ? i : i + 21) : i < 21 ? 56 + e2b + (i - 14) : 56 + e2b + nk - 15;
      return __builtin_amdgcn_raw_buffer_load_b128(rsWS, lane * 16, hb + kap * 1024, AUX_SC1);
    };
    bf16x8 dn0 = trfrag(dr0, dr0 + 1024), dn1 = trfrag(dr1, dr1 + 1024);
#pragma unroll
    for (int i = 0; i < HPF; ++i) { hf[i] = ldh(i); if (i == 0) { STAMP(5); } }
#pragma unroll
    for (int i = 0; i < 22; ++i) {
      __builtin_amdgcn_sched_barrier(0);
      if (i == 7) {
        LBAR();
        if (loader) { dmaDr(20, 21); if (nk == 22) dmaDr(21, 22); }
      }
      if (i == 7 + HPF + 1) LBAR();
      const bf16x8 d0 = dn0, d1 = dn1;
      if (i + 1 < 22) {
        const unsigned o = (i + 1 >= 20 ? i + 1 - 20 : i + 1) * 2048;
        dn0 = trfrag(dr0 + o, dr0 + o + 1024); dn1 = trfrag(dr1 + o, dr1 + o + 1024);
      }
      v4u hraw = hf[i % HPF];
      if (i == 21) { const unsigned msk = nk == 22 ? 0xffffffffu : 0u; hraw &= msk; }
      const bf16x8 hv = __builtin_bit_cast(bf16x8, hraw);
      acc2[0] = MFMA32(d0, hv, acc2[0]);
      acc2[1] = MFMA32(d1, hv, acc2[1]);
      if (i + HPF < 22) hf[i % HPF] = ldh(i + HPF);
      if (i == 7 + HPF + 1 && nt2 == 0 && w == 4 && lane == 0) {
        const v2u tg = {tagx, tagy};
        __builtin_amdgcn_raw_buffer_store_b64(tg, rsWS, 0, WS_DONE + mt2 * 8, AUX_SC1);
      }
    }
    __builtin_amdgcn_sched_barrier(0);
  }
  STAMP(6);
  v2u done0 = {0u, 0u};
  if (b == 0 && w == 4) done0 = __builtin_amdgcn_raw_buffer_load_b64(rsWS, lane * 8, WS_DONE, AUX_SC1);
  {
    const unsigned wa = ldsb + 4096 + (mbl * 4 + kq) * 2048 + lane * 16;
#pragma unroll
    for (int d = 0; d < 4; ++d)
#pragma unroll
      for (int g = 0; g < 2; ++g) {
        const int rb = 8 * (d & 1) + 4 * g;
        *(LAS f32x4*)(wa + d * SLOT + g * 1024) = f32x4{acc2[d >> 1][rb], acc2[d >> 1][rb + 1], acc2[d >> 1][rb + 2], acc2[d >> 1][rb + 3]};
      }
    const unsigned row = 64 * mt2 + 32 * mbl + (lane & 31);
    const unsigned col = 64 * nt2 + 16 * kq + 8 * hh;
    GAS v4u* op = (GAS v4u*)((GAS unsigned short*)po + row * D_ + col);
    LBAR();
    const unsigned ra = ldsb + kq * SLOT + 4096 + mbl * 8192 + lane * 16;
    f32x4 pv[4][2];
#pragma unroll
    for (int s = 0; s < 4; ++s)
#pragma unroll
      for (int g = 0; g < 2; ++g) pv[s][g] = *(LAS f32x4*)(ra + s * 2048 + g * 1024);
    const f32x4 s0 = (pv[0][0] + pv[1][0]) + (pv[2][0] + pv[3][0]), s1 = (pv[0][1] + pv[1][1]) + (pv[2][1] + pv[3][1]);
    const unsigned a0_ = cvtpk(f32x2{s0[0], s0[1]}), a1_ = cvtpk(f32x2{s0[2], s0[3]});
    const unsigned b0_ = cvtpk(f32x2{s1[0], s1[1]}), b1_ = cvtpk(f32x2{s1[2], s1[3]});
    auto t0 = __builtin_amdgcn_permlane32_swap(a0_, b0_, false, false);
    auto t1 = __builtin_amdgcn_permlane32_swap(a1_, b1_, false, false);
    *op = v4u{t0[0], t1[0], t0[1], t1[1]};
  }
  if (b == 0 && w == 4) {
    for (unsigned spin = 0; spin < (1u << 20); ++spin) {
      if (__all(lane >= 48 || (done0.x == tagx && done0.y == tagy))) break;
      asm volatile("" ::: "memory");
      done0 = __builtin_amdgcn_raw_buffer_load_b64(rsWS, lane * 8, WS_DONE, AUX_SC1);
    }
    if (lane == 0) { v2u tg = {tagx, tagy}; __builtin_amdgcn_raw_buffer_store_b64(tg, rsWS, 0, WS_EPOCH, AUX_SC1); }
  }
  STAMP(7);
  asm volatile(".Lffn_pf_end:");
}

extern "C" void kernel_launch(void* const* d_in, const int* in_sizes, int n_in, void* d_out, int out_size, void* d_ws, size_t ws_size,
                              hipStream_t stream) {
  (void)in_sizes; (void)n_in; (void)out_size; (void)ws_size;
  (void)hipFuncSetAttribute((const void*)ffn_fused, hipFuncAttributeMaxDynamicSharedMemorySize, 160 * 1024);
  ffn_fused<<<256, 512, 160 * 1024, stream>>>((const unsigned short*)d_in[0], (const unsigned short*)d_in[1], (const unsigned short*)d_in[2],
                                              (const unsigned short*)d_in[3], (unsigned short*)d_out, (unsigned char*)d_ws
                                              );
}
```

```cpp
#include <hip/hip_runtime.h>
#include <hip/hip_bf16.h>

typedef short bf16x8 __attribute__((ext_vector_type(8)));
typedef short s16x4 __attribute__((ext_vector_type(4)));
typedef float f32x4 __attribute__((ext_vector_type(4)));
typedef float f32x16 __attribute__((ext_vector_type(16)));
typedef float f32x2 __attribute__((ext_vector_type(2)));
typedef __bf16 bf16x2_t __attribute__((ext_vector_type(2)));
typedef unsigned v4u __attribute__((ext_vector_type(4)));
typedef unsigned v2u __attribute__((ext_vector_type(2)));
#define LAS __attribute__((address_space(3)))
#define GAS __attribute__((address_space(1)))
#define DI __device__ __forceinline__

constexpr int M_ = 2048, D_ = 512, F_ = 1376;
constexpr int WROW = F_ * 2;
constexpr int SLOT = 40960, XOFF = 16384, BOFF = 32768;
constexpr int NKS = F_ / 16;
constexpr int WS_EPOCH = 0;
constexpr int WS_DONE = 1024;
constexpr int WS_FLAGS = 4096;
constexpr int WS_H = 65536;
constexpr int WS_BYTES = WS_H + 64 * NKS * 1024;
constexpr int AUX_SC1 = 16;
#define HPF 7
#define DK1 1
#define PSLEEP 4
#define CODEPF 1

DI unsigned rfl(unsigned x) { return __builtin_amdgcn_readfirstlane(x); }
DI void dma16(unsigned m0v, unsigned voff, __amdgpu_buffer_rsrc_t rs, unsigned soff) {
  asm volatile("s_mov_b32 m0, %0\n\ts_nop 0\n\tbuffer_load_dwordx4 %1, %2, %3 offen lds"
               :: "s"(m0v), "v"(voff), "s"(rs), "s"(soff) : "memory");
}
DI void pf4(unsigned m0v, unsigned voff, __amdgpu_buffer_rsrc_t rs) {
  asm volatile("s_mov_b32 m0, %0\n\ts_nop 0\n\tbuffer_load_dword %1, %2, 0 offen lds" :: "s"(m0v), "v"(voff), "s"(rs) : "memory");
}
DI unsigned cvtpk(f32x2 v) {
  bf16x2_t b = __builtin_convertvector(v, bf16x2_t);
  return __builtin_bit_cast(unsigned, b);
}
DI bf16x8 trfrag(unsigned addr0, unsigned addr1) {
  s16x4 a = __builtin_amdgcn_ds_read_tr16_b64_v4i16((LAS s16x4*)addr0);
  s16x4 b = __builtin_amdgcn_ds_read_tr16_b64_v4i16((LAS s16x4*)addr1);
  return __builtin_shufflevector(a, b, 0, 1, 2, 3, 4, 5, 6, 7);
}
DI unsigned silu_mul2(f32x2 g, f32x2 u) {
  const f32x2 t = g * -1.44269504088896340736f;
  f32x2 e = {__builtin_amdgcn_exp2f(t.x), __builtin_amdgcn_exp2f(t.y)};
  e = e + 1.0f;
  const f32x2 rc = {__builtin_amdgcn_rcpf(e.x), __builtin_amdgcn_rcpf(e.y)};
  return cvtpk((g * u) * rc);
}
#define WAIT_V(N) asm volatile("s_waitcnt vmcnt(" #N ")" ::: "memory")
#define BAR() asm volatile("s_barrier" ::: "memory")
#define LBAR() asm volatile("s_waitcnt lgkmcnt(0)\n\ts_barrier" ::: "memory")
#define VBAR(N) asm volatile("s_waitcnt vmcnt(" #N ")\n\ts_barrier" ::: "memory")
#define MFMA32(a, b, c) __builtin_amdgcn_mfma_f32_32x32x16_bf16((a), (b), (c), 0, 0, 0)
#define SGB(mask, n) __builtin_amdgcn_sched_group_barrier((mask), (n), 0)

#define STAMP(i)

struct Frags { bf16x8 x; bf16x8 wv[2][3]; };

__global__ __launch_bounds__(512, 2) void ffn_fused(const unsigned short* __restrict__ x, const unsigned short* __restrict__ wg,
                                                    const unsigned short* __restrict__ wu, const unsigned short* __restrict__ wd,
                                                    unsigned short* __restrict__ out, unsigned char* __restrict__ ws
                                                    ) {
  extern __shared__ __attribute__((aligned(16))) char lds_[];
  const unsigned ldsb = (unsigned)(size_t)lds_;
  const unsigned tid = threadIdx.x, lane = tid & 63;
  const unsigned w = rfl(tid >> 6);
  const unsigned b = blockIdx.x, xcd = b & 7, r = b >> 3;
  STAMP(0);

  const unsigned pmi = r & 7, pfi = r >> 3;
  const unsigned pm = 8 * (xcd & 1) + pmi, pf = 4 * (xcd >> 1) + pfi;
  const bool producer = pf < 15;
  const unsigned f0 = pf < 14 ? 96 * pf : 1280;
  const bool loader = w < 4;
  const unsigned kq = w & 3, mbl = w >> 2;

  unsigned long long px = (unsigned long long)x, pw = (unsigned long long)(kq < 2 ? wg : wu), pd = (unsigned long long)wd,
                     po = (unsigned long long)out, pws = (unsigned long long)ws;
  asm volatile("" : "+s"(px), "+s"(pw), "+s"(pd), "+s"(po), "+s"(pws));
  unsigned long long epoch = *(const __attribute__((address_space(4))) unsigned long long*)(pws + WS_EPOCH);

  if (loader && producer) {
    const unsigned lw = w;
    const __amdgpu_buffer_rsrc_t rsX = __builtin_amdgcn_make_buffer_rsrc((void*)px, (short)0, M_ * D_ * 2, 0x00020000);
    const __amdgpu_buffer_rsrc_t rsW = __builtin_amdgcn_make_buffer_rsrc((void*)pw, (short)0, D_ * F_ * 2, 0x00020000);
    const unsigned rot1 = (4 * (pmi & 1) + 2 * (xcd >> 1) + (xcd & 1)) & 7;
    const unsigned voffX0 = (lane >> 3) * 1024 + (((lane & 7) ^ (lane >> 4)) << 4);
    const unsigned voffX1 = voffX0 ^ 64;
    const unsigned voffW = (lane >> 2) * WROW + ((lane & 3) << 4);
    const unsigned sX = (pm * 128 + 16 * lw) * 1024;
    const unsigned sW = ((lw & 1) * 16) * WROW + f0 * 2;
    const unsigned mX = ldsb + XOFF + 16 * lw * 128;
    const unsigned mA = ldsb + (lw >> 1) * 4096 + (lw & 1) * 1024;
    const unsigned mB = ldsb + BOFF + (lw >> 1) * 2048 + (lw & 1) * 1024;
    auto dmaX = [&](unsigned xh) {
      const unsigned j = xh >> 1, h = xh & 1, sl = (j + rot1) & 7;
      const unsigned m0 = mX + (j & 3) * SLOT + h * 8192, so = sX + h * 65536 + sl * 128;
      dma16(m0, voffX0, rsX, so);
      dma16(m0 + 1024, voffX1, rsX, so + 8192);
    };
    auto dmaWA = [&](unsigned p) {
      const unsigned j = p >> 1, h = p & 1, sl = (j + rot1) & 7;
      const unsigned m0 = mA + (j & 3) * SLOT + h * 8192, so = sW + (sl * 64 + h * 32) * WROW;
      dma16(m0, voffW, rsW, so);
      dma16(m0 + 2048, voffW, rsW, so + 64);
    };
    auto dmaWB = [&](unsigned p) {
      const unsigned j = p >> 1, h = p & 1, sl = (j + rot1) & 7;
      dma16(mB + (j & 3) * SLOT + h * 4096, voffW, rsW, sW + (sl * 64 + h * 32) * WROW + 128);
    };
    dmaWA(0); dmaWB(0); dmaX(0); dmaX(1);
    dmaWA(1); dmaWB(1); dmaX(2);
    dmaWA(2); dmaWB(2); dmaX(3);
    asm volatile("" :: "s"(epoch));
    STAMP(1);
#pragma unroll 1
    for (unsigned p = 0; p < 5; ++p) {
      VBAR(10);
      dmaWA(p + 3); dmaWB(p + 3); dmaX(p + 4);
    }
#pragma unroll 1
    for (unsigned p = 5; p < 12; ++p) {
      VBAR(8);
      dmaWA(p + 3); dmaX(p + 4);
    }
    VBAR(8);
    dmaWA(15);
    VBAR(6);
    dmaWB(8); dmaWB(9); dmaWB(10); dmaWB(11);
    VBAR(6);
    dmaWB(12); dmaWB(13); dmaWB(14); dmaWB(15);
    VBAR(8);
    BAR();
    STAMP(2);
  }

  const __amdgpu_buffer_rsrc_t rsD = __builtin_amdgcn_make_buffer_rsrc((void*)pd, (short)0, F_ * D_ * 2, 0x00020000);
  const __amdgpu_buffer_rsrc_t rsWS = __builtin_amdgcn_make_buffer_rsrc((void*)pws, (short)0, WS_BYTES, 0x00020000);
  const unsigned mt2 = 4 * xcd + (r >> 3), nt2 = r & 7;
  const unsigned nk = kq < 2 ? 22 : 21;
  const unsigned e2b = kq * 8 - (kq >> 1 & kq & 1);
  const unsigned voffD = (lane >> 3) * 1024 + (((lane & 7) ^ (((lane >> 4) & 1) << 2)) << 4);
  auto dmaDr = [&](int i0, int i1) {
#pragma unroll
    for (int part = 0; part < 3; ++part) {
      const int lo = i0 > 7 * part ? i0 : 7 * part, hi = (part < 2 && i1 > 7 * part + 7) ? 7 * part + 7 : i1;
      if (lo < hi) {
        const unsigned e = (part < 2 ? 7 * kq : e2b) + (lo - 7 * part);
        unsigned m0 = ldsb + kq * SLOT + (lo >= 20 ? lo - 20 : lo) * 2048;
        unsigned so = (6 * (e >> 1) + (e & 1) + 2 * part) * 16384 + nt2 * 128;
        unsigned st = 16384 + (e & 1) * 65536;
#pragma unroll 1
        for (int i = lo; i < hi; ++i) {
          const unsigned sq = (part == 2 && so >= 88 * 16384) ? so - 65536 : so;
          dma16(m0, voffD, rsD, sq);
          dma16(m0 + 1024, voffD, rsD, sq + 8192);
          m0 += 2048; so += st; st ^= 65536;
        }
      }
    }
  };

  asm volatile("" : "+s"(epoch));
  const unsigned long long tag64 = epoch + 1ull;
  const unsigned tagx = (unsigned)tag64, tagy = (unsigned)(tag64 >> 32);

  if (loader) {
    dmaDr(0, DK1);
    STAMP(8);
    if (producer) asm volatile("s_waitcnt vmcnt(%0)\n\ts_barrier" :: "n"(2 * DK1) : "memory");
    STAMP(9);
    dmaDr(DK1, 16);
    STAMP(10);
    VBAR(16);
    STAMP(3);
    dmaDr(16, 20);
    STAMP(4);
  } else {
    if (w == 4 && producer) {
      unsigned long long pc;
      unsigned len;
      asm volatile("s_getpc_b64 %0\n.Lffn_pf_here:\n\ts_mov_b32 %1, .Lffn_pf_end-.Lffn_pf_here" : "=s"(pc), "=s"(len));
      pf4(ldsb + 3 * SLOT, lane < 4 ? r * 512 + lane * 128 : 0x40000000u, __builtin_amdgcn_make_buffer_rsrc((void*)pc, (short)0, (int)len, 0x00020000));
    }
    const v2u tg = {tagx, tagy};
    if (producer) {
      const unsigned mbw = w & 3;
      const unsigned r32 = lane & 31, h2 = lane >> 5;
      unsigned xr[4];
#pragma unroll
      for (int c2 = 0; c2 < 4; ++c2) xr[c2] = ldsb + XOFF + (32 * mbw + r32) * 128 + (((2 * c2 + h2) ^ ((r32 >> 1) & 7)) << 4);
      const unsigned wr = ldsb + (8 * h2 + ((lane & 15) >> 2)) * 64 + ((lane >> 4) & 1) * 32 + 8 * (lane & 3);

      f32x16 acc[2][3];
#pragma unroll
      for (int a = 0; a < 2; ++a)
#pragma unroll
        for (int j = 0; j < 3; ++j) {
#pragma unroll
          for (int i = 0; i < 16; ++i) acc[a][j][i] = 0.f;
          asm volatile("" : "+v"(acc[a][j]));
        }
      auto rd = [&](Frags& Fr, unsigned slot, int h, int k, int mode) {
        Fr.x = *(LAS bf16x8*)(xr[2 * h + k] + slot);
#pragma unroll
        for (int a = 0; a < 2; ++a) {
          if (mode & 1) {
#pragma unroll
            for (int j = 0; j < 2; ++j) {
              const unsigned ad = wr + slot + h * 8192 + a * 4096 + j * 2048 + k * 1024;
              Fr.wv[a][j] = trfrag(ad, ad + 256);
            }
          }
          if (mode & 2) {
            const unsigned ad = wr + slot + BOFF + h * 4096 + a * 2048 + k * 1024;
            Fr.wv[a][2] = trfrag(ad, ad + 256);
          }
        }
      };
      auto mm = [&](const Frags& Fr, int mode) {
#pragma unroll
        for (int a = 0; a < 2; ++a)
#pragma unroll
          for (int j = 0; j < 3; ++j)
            if (j < 2 ? (mode & 1) : (mode & 2)) acc[a][j] = MFMA32(Fr.wv[a][j], Fr.x, acc[a][j]);
      };
      auto il = [&](int nm) {
        SGB(0x008, 1); SGB(0x100, 3);
#pragma unroll
        for (int i = 1; i < nm; ++i) { SGB(0x008, 1); SGB(0x100, 2); }
        __builtin_amdgcn_sched_barrier(0);
      };
      const unsigned hoff = lane * 16 + WS_H + ((4 * pm + mbw) * NKS + 2 * pf) * 1024;
      auto epi = [&](int j) {
#pragma unroll
        for (int sx = 0; sx < 2; ++sx) {
          v4u o;
#pragma unroll
          for (int e = 0; e < 4; ++e) {
            const int i0 = 8 * sx + 2 * e;
            o[e] = silu_mul2(f32x2{acc[0][j][i0], acc[0][j][i0 + 1]}, f32x2{acc[1][j][i0], acc[1][j][i0 + 1]});
          }
          __builtin_amdgcn_raw_buffer_store_b128(o, rsWS, hoff + (28 * j + sx) * 1024, 0, AUX_SC1);
        }
      };
      Frags A, B;
      __builtin_amdgcn_s_setprio(1);
      BAR();
      STAMP(1);
      rd(A, 0, 0, 0, 3);
#pragma unroll 1
      for (unsigned j = 0; j < 4; ++j) {
        const unsigned slot = j * SLOT, slotn = ((j + 1) & 3) * SLOT;
        rd(B, slot, 0, 1, 3); mm(A, 3); il(6);
        if (j == 1) WAIT_V(0);
        BAR();
        rd(A, slot, 1, 0, 3); mm(B, 3); il(6);
        rd(B, slot, 1, 1, 3); mm(A, 3); il(6);
        BAR();
        rd(A, slotn, 0, 0, 3);
        mm(B, 3); il(6);
      }
      bf16x8 xk[16];
#pragma unroll
      for (int j = 0; j < 4; ++j) {
        const unsigned slot = j * SLOT;
        xk[4 * j] = A.x;
        rd(B, slot, 0, 1, 1); mm(A, 1); il(4);
        BAR();
        xk[4 * j + 1] = B.x;
        rd(A, slot, 1, 0, 1); mm(B, 1); il(4);
        xk[4 * j + 2] = A.x;
        rd(B, slot, 1, 1, 1); mm(A, 1); il(4);
        if (j == 3) asm volatile("s_waitcnt lgkmcnt(0)" ::: "memory");
        BAR();
        xk[4 * j + 3] = B.x;
        if (j < 3) { rd(A, slot + SLOT, 0, 0, 1); mm(B, 1); il(4); }
        else mm(B, 1);
      }
      STAMP(2);
      if (pf < 14) { epi(0); epi(1); }
      STAMP(8);
      BAR();
      STAMP(9);
      {
        auto rdb = [&](bf16x8 (&wb)[2], int t) {
#pragma unroll
          for (int a = 0; a < 2; ++a) {
            const unsigned ad = wr + (t >> 2) * SLOT + BOFF + ((t >> 1) & 1) * 4096 + a * 2048 + (t & 1) * 1024;
            wb[a] = trfrag(ad, ad + 256);
          }
        };
        bf16x8 wb[3][2];
        rdb(wb[0], 0); rdb(wb[1], 1);
#pragma unroll
        for (int t = 0; t < 16; ++t) {
          if (t + 2 < 16) rdb(wb[(t + 2) % 3], t + 2);
          acc[0][2] = MFMA32(wb[t % 3][0], xk[t], acc[0][2]);
          acc[1][2] = MFMA32(wb[t % 3][1], xk[t], acc[1][2]);
          SGB(0x008, 1); SGB(0x100, 2); SGB(0x008, 1); SGB(0x100, 2);
          __builtin_amdgcn_sched_barrier(0);
        }
      }
      STAMP(10);
      const unsigned fo = WS_FLAGS + ((4 * pm + mbw) * 32 + pf) * 8;
      WAIT_V(0);
      if (lane == 0 && pf < 14) __builtin_amdgcn_raw_buffer_store_b64(tg, rsWS, 0, fo, AUX_SC1);
      STAMP(11);
      epi(2);
      __builtin_amdgcn_s_setprio(0);
      STAMP(12);
      WAIT_V(0);
      if (lane == 0) __builtin_amdgcn_raw_buffer_store_b64(tg, rsWS, 0, fo + 128, AUX_SC1);
      STAMP(3);
      LBAR();
    } else {
      BAR();
      if (w == 4 && lane == 0) __builtin_amdgcn_raw_buffer_store_b64(tg, rsWS, 0, WS_DONE + (32 + (xcd & 1) * 8 + pmi) * 8, AUX_SC1);
    }
    STAMP(4);
  }

  const unsigned hh = lane >> 5, blk = (lane >> 4) & 1, q2 = (lane & 15) >> 2, p2 = lane & 3;
  const unsigned dr0 = ldsb + kq * SLOT + (4 * hh + q2) * 128 + ((q2 >> 1) * 64) + blk * 32 + 8 * p2;
  const unsigned dr1 = dr0 ^ 64;
  f32x16 acc2[2];
#pragma unroll
  for (int i = 0; i < 16; ++i) { acc2[0][i] = 0.f; acc2[1][i] = 0.f; }
  asm volatile("" : "+v"(acc2[0]), "+v"(acc2[1]));
  {
    const unsigned mb2 = 2 * mt2 + mbl;
    const unsigned hb = WS_H + (mb2 * NKS) * 1024;
    const unsigned foff = WS_FLAGS + (mb2 * 32 + (lane & 31)) * 8;
    const unsigned loA = (7 * kq) >> 1, hiA = (7 * kq + 6) >> 1, loB = e2b >> 1, hiB = (e2b + nk - 15) >> 1;
    const unsigned needA = ((2u << hiA) - 1u) & ~((1u << loA) - 1u);
    const unsigned needB = (((2u << hiB) - 1u) & ~((1u << loB) - 1u)) << 16;
    unsigned seen = 0;
    auto wait_flags = [&](unsigned need) {
      for (unsigned spin = 0; (seen & need) != need && spin < (1u << 20); ++spin) {
        asm volatile("" ::: "memory");
        const v2u f = __builtin_amdgcn_raw_buffer_load_b64(rsWS, foff, 0, AUX_SC1);
        seen = (unsigned)__builtin_amdgcn_ballot_w64(f.x == tagx && f.y == tagy);
        if ((seen & need) != need) __builtin_amdgcn_s_sleep(PSLEEP);
      }
      __builtin_amdgcn_fence(__ATOMIC_ACQUIRE, "wavefront");
    };
    v4u hf[HPF];
    auto ldh = [&](int i) {
      if (i == 0) wait_flags(needA);
      if (i == 14) wait_flags(needB);
      const unsigned kap = i < 14 ? 7 * kq + (i < 7 ? i : i + 21) : i < 21 ? 56 + e2b + (i - 14) : 56 + e2b + nk - 15;
      return __builtin_amdgcn_raw_buffer_load_b128(rsWS, lane * 16, hb + kap * 1024, AUX_SC1);
    };
    bf16x8 dn0 = trfrag(dr0, dr0 + 1024), dn1 = trfrag(dr1, dr1 + 1024);
#pragma unroll
    for (int i = 0; i < HPF; ++i) { hf[i] = ldh(i); if (i == 0) { STAMP(5); } }
#pragma unroll
    for (int i = 0; i < 22; ++i) {
      __builtin_amdgcn_sched_barrier(0);
      if (i == 7) {
        LBAR();
        if (loader) { dmaDr(20, 21); if (nk == 22) dmaDr(21, 22); }
      }
      if (i == 7 + HPF + 1) LBAR();
      const bf16x8 d0 = dn0, d1 = dn1;
      if (i + 1 < 22) {
        const unsigned o = (i + 1 >= 20 ? i + 1 - 20 : i + 1) * 2048;
        dn0 = trfrag(dr0 + o, dr0 + o + 1024); dn1 = trfrag(dr1 + o, dr1 + o + 1024);
      }
      v4u hraw = hf[i % HPF];
      if (i == 21) { const unsigned msk = nk == 22 ? 0xffffffffu : 0u; hraw &= msk; }
      const bf16x8 hv = __builtin_bit_cast(bf16x8, hraw);
      acc2[0] = MFMA32(d0, hv, acc2[0]);
      acc2[1] = MFMA32(d1, hv, acc2[1]);
      if (i + HPF < 22) hf[i % HPF] = ldh(i + HPF);
      if (i == 7 + HPF + 1 && nt2 == 0 && w == 4 && lane == 0) {
        const v2u tg = {tagx, tagy};
        __builtin_amdgcn_raw_buffer_store_b64(tg, rsWS, 0, WS_DONE + mt2 * 8, AUX_SC1);
      }
    }
    __builtin_amdgcn_sched_barrier(0);
  }
  STAMP(6);
  {
    const unsigned wa = ldsb + 4096 + (mbl * 4 + kq) * 2048 + lane * 16;
#pragma unroll
    for (int d = 0; d < 4; ++d)
#pragma unroll
      for (int g = 0; g < 2; ++g) {
        const int rb = 8 * (d & 1) + 4 * g;
        *(LAS f32x4*)(wa + d * SLOT + g * 1024) = f32x4{acc2[d >> 1][rb], acc2[d >> 1][rb + 1], acc2[d >> 1][rb + 2], acc2[d >> 1][rb + 3]};
      }
    const unsigned row = 64 * mt2 + 32 * mbl + (lane & 31);
    const unsigned col = 64 * nt2 + 16 * kq + 8 * hh;
    GAS v4u* op = (GAS v4u*)((GAS unsigned short*)po + row * D_ + col);
    LBAR();
    const unsigned ra = ldsb + kq * SLOT + 4096 + mbl * 8192 + lane * 16;
    f32x4 pv[4][2];
#pragma unroll
    for (int s = 0; s < 4; ++s)
#pragma unroll
      for (int g = 0; g < 2; ++g) pv[s][g] = *(LAS f32x4*)(ra + s * 2048 + g * 1024);
    const f32x4 s0 = (pv[0][0] + pv[1][0]) + (pv[2][0] + pv[3][0]), s1 = (pv[0][1] + pv[1][1]) + (pv[2][1] + pv[3][1]);
    const unsigned a0_ = cvtpk(f32x2{s0[0], s0[1]}), a1_ = cvtpk(f32x2{s0[2], s0[3]});
    const unsigned b0_ = cvtpk(f32x2{s1[0], s1[1]}), b1_ = cvtpk(f32x2{s1[2], s1[3]});
    auto t0 = __builtin_amdgcn_permlane32_swap(a0_, b0_, false, false);
    auto t1 = __builtin_amdgcn_permlane32_swap(a1_, b1_, false, false);
    *op = v4u{t0[0], t1[0], t0[1], t1[1]};
  }
  if (b == 0 && w == 4) {
    for (unsigned spin = 0; spin < (1u << 20); ++spin) {
      asm volatile("" ::: "memory");
      const v2u done = __builtin_amdgcn_raw_buffer_load_b64(rsWS, lane * 8, WS_DONE, AUX_SC1);
      if (__all(lane >= 48 || (done.x == tagx && done.y == tagy))) break;
    }
    if (lane == 0) { v2u tg = {tagx, tagy}; __builtin_amdgcn_raw_buffer_store_b64(tg, rsWS, 0, WS_EPOCH, AUX_SC1); }
  }
  STAMP(7);
  asm volatile(".Lffn_pf_end:");
}

extern "C" void kernel_launch(void* const* d_in, const int* in_sizes, int n_in, void* d_out, int out_size, void* d_ws, size_t ws_size,
                              hipStream_t stream) {
  (void)in_sizes; (void)n_in; (void)out_size; (void)ws_size;
  (void)hipFuncSetAttribute((const void*)ffn_fused, hipFuncAttributeMaxDynamicSharedMemorySize, 160 * 1024);
  ffn_fused<<<256, 512, 160 * 1024, stream>>>((const unsigned short*)d_in[0], (const unsigned short*)d_in[1], (const unsigned short*)d_in[2],
                                              (const unsigned short*)d_in[3], (unsigned short*)d_out, (unsigned char*)d_ws
                                              );
}
```

```cpp
#include <hip/hip_runtime.h>
#include <hip/hip_bf16.h>

typedef short bf16x8 __attribute__((ext_vector_type(8)));
typedef short s16x4 __attribute__((ext_vector_type(4)));
typedef float f32x4 __attribute__((ext_vector_type(4)));
typedef float f32x16 __attribute__((ext_vector_type(16)));
typedef float f32x2 __attribute__((ext_vector_type(2)));
typedef __bf16 bf16x2_t __attribute__((ext_vector_type(2)));
typedef unsigned v4u __attribute__((ext_vector_type(4)));
typedef unsigned v2u __attribute__((ext_vector_type(2)));
#define LAS __attribute__((address_space(3)))
#define GAS __attribute__((address_space(1)))
#define DI __device__ __forceinline__

constexpr int M_ = 2048, D_ = 512, F_ = 1376;
constexpr int WROW = F_ * 2;
constexpr int SLOT = 40960, XOFF = 16384, BOFF = 32768;
constexpr int NKS = F_ / 16;
constexpr int WS_EPOCH = 0;
constexpr int WS_DONE = 4096;
constexpr int WS_FLAGS = 16384;
constexpr int WS_H = 16384 + 262144;
constexpr int WS_BYTES = WS_H + 64 * NKS * 1024;
constexpr int AUX_SC1 = 16;
#define HPF 7
#define DK1 1
#define PSLEEP 4
#define POLLALL 0
#define CODEPF 1

DI unsigned rfl(unsigned x) { return __builtin_amdgcn_readfirstlane(x); }
DI void dma16(unsigned m0v, unsigned voff, __amdgpu_buffer_rsrc_t rs, unsigned soff) {
  asm volatile("s_mov_b32 m0, %0\n\ts_nop 0\n\tbuffer_load_dwordx4 %1, %2, %3 offen lds"
               :: "s"(m0v), "v"(voff), "s"(rs), "s"(soff) : "memory");
}
DI void pf4(unsigned m0v, unsigned voff, __amdgpu_buffer_rsrc_t rs) {
  asm volatile("s_mov_b32 m0, %0\n\ts_nop 0\n\tbuffer_load_dword %1, %2, 0 offen lds" :: "s"(m0v), "v"(voff), "s"(rs) : "memory");
}
DI unsigned cvtpk(f32x2 v) {
  bf16x2_t b = __builtin_convertvector(v, bf16x2_t);
  return __builtin_bit_cast(unsigned, b);
}
DI bf16x8 trfrag(unsigned addr0, unsigned addr1) {
  s16x4 a = __builtin_amdgcn_ds_read_tr16_b64_v4i16((LAS s16x4*)addr0);
  s16x4 b = __builtin_amdgcn_ds_read_tr16_b64_v4i16((LAS s16x4*)addr1);
  return __builtin_shufflevector(a, b, 0, 1, 2, 3, 4, 5, 6, 7);
}
DI unsigned silu_mul2(f32x2 g, f32x2 u) {
  const f32x2 t = g * -1.44269504088896340736f;
  f32x2 e = {__builtin_amdgcn_exp2f(t.x), __builtin_amdgcn_exp2f(t.y)};
  e = e + 1.0f;
  const f32x2 rc = {__builtin_amdgcn_rcpf(e.x), __builtin_amdgcn_rcpf(e.y)};
  return cvtpk((g * u) * rc);
}
#define WAIT_V(N) asm volatile("s_waitcnt vmcnt(" #N ")" ::: "memory")
#define BAR() asm volatile("s_barrier" ::: "memory")
#define LBAR() asm volatile("s_waitcnt lgkmcnt(0)\n\ts_barrier" ::: "memory")
#define VBAR(N) asm volatile("s_waitcnt vmcnt(" #N ")\n\ts_barrier" ::: "memory")
#define MFMA32(a, b, c) __builtin_amdgcn_mfma_f32_32x32x16_bf16((a), (b), (c), 0, 0, 0)
#define SGB(mask, n) __builtin_amdgcn_sched_group_barrier((mask), (n), 0)

#define STAMP(i)

struct Frags { bf16x8 x; bf16x8 wv[2][3]; };

__global__ __launch_bounds__(512, 2) void ffn_fused(const unsigned short* __restrict__ x, const unsigned short* __restrict__ wg,
                                                    const unsigned short* __restrict__ wu, const unsigned short* __restrict__ wd,
                                                    unsigned short* __restrict__ out, unsigned char* __restrict__ ws
                                                    ) {
  extern __shared__ __attribute__((aligned(16))) char lds_[];
  const unsigned ldsb = (unsigned)(size_t)lds_;
  const unsigned tid = threadIdx.x, lane = tid & 63;
  const unsigned w = rfl(tid >> 6);
  const unsigned b = blockIdx.x, xcd = b & 7, r = b >> 3;
  STAMP(0);

  const unsigned pmi = r & 7, pfi = r >> 3;
  const unsigned pm = 8 * (xcd & 1) + pmi, pf = 4 * (xcd >> 1) + pfi;
  const bool producer = pf < 15;
  const unsigned f0 = pf < 14 ? 96 * pf : 1280;
  const bool loader = w < 4;
  const unsigned kq = w & 3, mbl = w >> 2;

  unsigned long long px = (unsigned long long)x, pw = (unsigned long long)(kq < 2 ? wg : wu), pd = (unsigned long long)wd,
                     po = (unsigned long long)out, pws = (unsigned long long)ws;
  asm volatile("" : "+s"(px), "+s"(pw), "+s"(pd), "+s"(po), "+s"(pws));
  unsigned long long epoch = *(const __attribute__((address_space(4))) unsigned long long*)(pws + WS_EPOCH);

  if (loader && producer) {
    const unsigned lw = w;
    const __amdgpu_buffer_rsrc_t rsX = __builtin_amdgcn_make_buffer_rsrc((void*)px, (short)0, M_ * D_ * 2, 0x00020000);
    const __amdgpu_buffer_rsrc_t rsW = __builtin_amdgcn_make_buffer_rsrc((void*)pw, (short)0, D_ * F_ * 2, 0x00020000);
    const unsigned rot1 = (4 * (pmi & 1) + 2 * (xcd >> 1) + (xcd & 1)) & 7;
    const unsigned voffX0 = (lane >> 3) * 1024 + (((lane & 7) ^ (lane >> 4)) << 4);
    const unsigned voffX1 = voffX0 ^ 64;
    const unsigned voffW = (lane >> 2) * WROW + ((lane & 3) << 4);
    const unsigned sX = (pm * 128 + 16 * lw) * 1024;
    const unsigned sW = ((lw & 1) * 16) * WROW + f0 * 2;
    const unsigned mX = ldsb + XOFF + 16 * lw * 128;
    const unsigned mA = ldsb + (lw >> 1) * 4096 + (lw & 1) * 1024;
    const unsigned mB = ldsb + BOFF + (lw >> 1) * 2048 + (lw & 1) * 1024;
    auto dmaX = [&](unsigned xh) {
      const unsigned j = xh >> 1, h = xh & 1, sl = (j + rot1) & 7;
      const unsigned m0 = mX + (j & 3) * SLOT + h * 8192, so = sX + h * 65536 + sl * 128;
      dma16(m0, voffX0, rsX, so);
      dma16(m0 + 1024, voffX1, rsX, so + 8192);
    };
    auto dmaWA = [&](unsigned p) {
      const unsigned j = p >> 1, h = p & 1, sl = (j + rot1) & 7;
      const unsigned m0 = mA + (j & 3) * SLOT + h * 8192, so = sW + (sl * 64 + h * 32) * WROW;
      dma16(m0, voffW, rsW, so);
      dma16(m0 + 2048, voffW, rsW, so + 64);
    };
    auto dmaWB = [&](unsigned p) {
      const unsigned j = p >> 1, h = p & 1, sl = (j + rot1) & 7;
      dma16(mB + (j & 3) * SLOT + h * 4096, voffW, rsW, sW + (sl * 64 + h * 32) * WROW + 128);
    };
    dmaWA(0); dmaWB(0); dmaX(0); dmaX(1);
    dmaWA(1); dmaWB(1); dmaX(2);
    dmaWA(2); dmaWB(2); dmaX(3);
    asm volatile("" :: "s"(epoch));
    STAMP(1);
#pragma unroll 1
    for (unsigned p = 0; p < 5; ++p) {
      VBAR(10);
      dmaWA(p + 3); dmaWB(p + 3); dmaX(p + 4);
    }
#pragma unroll 1
    for (unsigned p = 5; p < 12; ++p) {
      VBAR(8);
      dmaWA(p + 3); dmaX(p + 4);
    }
    VBAR(8);
    dmaWA(15);
    VBAR(6);
    dmaWB(8); dmaWB(9); dmaWB(10); dmaWB(11);
    VBAR(6);
    dmaWB(12); dmaWB(13); dmaWB(14); dmaWB(15);
    VBAR(8);
    BAR();
    STAMP(2);
  }

  const __amdgpu_buffer_rsrc_t rsD = __builtin_amdgcn_make_buffer_rsrc((void*)pd, (short)0, F_ * D_ * 2, 0x00020000);
  const __amdgpu_buffer_rsrc_t rsWS = __builtin_amdgcn_make_buffer_rsrc((void*)pws, (short)0, WS_BYTES, 0x00020000);
  const unsigned mt2 = 4 * xcd + (r >> 3), nt2 = r & 7;
  const unsigned nk = kq < 2 ? 22 : 21;
  const unsigned e2b = kq * 8 - (kq >> 1 & kq & 1);
  const unsigned voffD = (lane >> 3) * 1024 + (((lane & 7) ^ (((lane >> 4) & 1) << 2)) << 4);
  auto dmaDr = [&](int i0, int i1) {
#pragma unroll
    for (int part = 0; part < 3; ++part) {
      const int lo = i0 > 7 * part ? i0 : 7 * part, hi = (part < 2 && i1 > 7 * part + 7) ? 7 * part + 7 : i1;
      if (lo < hi) {
        const unsigned e = (part < 2 ? 7 * kq : e2b) + (lo - 7 * part);
        unsigned m0 = ldsb + kq * SLOT + (lo >= 20 ? lo - 20 : lo) * 2048;
        unsigned so = (6 * (e >> 1) + (e & 1) + 2 * part) * 16384 + nt2 * 128;
        unsigned st = 16384 + (e & 1) * 65536;
#pragma unroll 1
        for (int i = lo; i < hi; ++i) {
          const unsigned sq = (part == 2 && so >= 88 * 16384) ? so - 65536 : so;
          dma16(m0, voffD, rsD, sq);
          dma16(m0 + 1024, voffD, rsD, sq + 8192);
          m0 += 2048; so += st; st ^= 65536;
        }
      }
    }
  };

  asm volatile("" : "+s"(epoch));
  const unsigned long long tag64 = epoch + 1ull;
  const unsigned tagx = (unsigned)tag64, tagy = (unsigned)(tag64 >> 32);

  if (loader) {
    dmaDr(0, DK1);
    STAMP(8);
    if (producer) asm volatile("s_waitcnt vmcnt(%0)\n\ts_barrier" :: "n"(2 * DK1) : "memory");
    STAMP(9);
    dmaDr(DK1, 9);
    if (producer) BAR();
    dmaDr(9, 16);
    STAMP(10);
    VBAR(16);
    STAMP(3);
    dmaDr(16, 20);
    STAMP(4);
  } else {
    if (w == 4 && producer) {
      unsigned long long pc;
      unsigned len;
      asm volatile("s_getpc_b64 %0\n.Lffn_pf_here:\n\ts_mov_b32 %1, .Lffn_pf_end-.Lffn_pf_here" : "=s"(pc), "=s"(len));
      pf4(ldsb + 3 * SLOT, lane < 4 ? r * 512 + lane * 128 : 0x40000000u, __builtin_amdgcn_make_buffer_rsrc((void*)pc, (short)0, (int)len, 0x00020000));
    }
    const v2u tg = {tagx, tagy};
    if (producer) {
      const unsigned mbw = w & 3;
      const unsigned r32 = lane & 31, h2 = lane >> 5;
      unsigned xr[4];
#pragma unroll
      for (int c2 = 0; c2 < 4; ++c2) xr[c2] = ldsb + XOFF + (32 * mbw + r32) * 128 + (((2 * c2 + h2) ^ ((r32 >> 1) & 7)) << 4);
      const unsigned wr = ldsb + (8 * h2 + ((lane & 15) >> 2)) * 64 + ((lane >> 4) & 1) * 32 + 8 * (lane & 3);

      f32x16 acc[2][3];
#pragma unroll
      for (int a = 0; a < 2; ++a)
#pragma unroll
        for (int j = 0; j < 3; ++j) {
#pragma unroll
          for (int i = 0; i < 16; ++i) acc[a][j][i] = 0.f;
          asm volatile("" : "+v"(acc[a][j]));
        }
      auto rd = [&](Frags& Fr, unsigned slot, int h, int k, int mode) {
        Fr.x = *(LAS bf16x8*)(xr[2 * h + k] + slot);
#pragma unroll
        for (int a = 0; a < 2; ++a) {
          if (mode & 1) {
#pragma unroll
            for (int j = 0; j < 2; ++j) {
              const unsigned ad = wr + slot + h * 8192 + a * 4096 + j * 2048 + k * 1024;
              Fr.wv[a][j] = trfrag(ad, ad + 256);
            }
          }
          if (mode & 2) {
            const unsigned ad = wr + slot + BOFF + h * 4096 + a * 2048 + k * 1024;
            Fr.wv[a][2] = trfrag(ad, ad + 256);
          }
        }
      };
      auto mm = [&](const Frags& Fr, int mode) {
#pragma unroll
        for (int a = 0; a < 2; ++a)
#pragma unroll
          for (int j = 0; j < 3; ++j)
            if (j < 2 ? (mode & 1) : (mode & 2)) acc[a][j] = MFMA32(Fr.wv[a][j], Fr.x, acc[a][j]);
      };
      auto il = [&](int nm) {
        SGB(0x008, 1); SGB(0x100, 3);
#pragma unroll
        for (int i = 1; i < nm; ++i) { SGB(0x008, 1); SGB(0x100, 2); }
        __builtin_amdgcn_sched_barrier(0);
      };
      const unsigned hoff = lane * 16 + WS_H + ((4 * pm + mbw) * NKS + 2 * pf) * 1024;
      auto epi = [&](int j) {
#pragma unroll
        for (int sx = 0; sx < 2; ++sx) {
          v4u o;
#pragma unroll
          for (int e = 0; e < 4; ++e) {
            const int i0 = 8 * sx + 2 * e;
            o[e] = silu_mul2(f32x2{acc[0][j][i0], acc[0][j][i0 + 1]}, f32x2{acc[1][j][i0], acc[1][j][i0 + 1]});
          }
          __builtin_amdgcn_raw_buffer_store_b128(o, rsWS, hoff + (28 * j + sx) * 1024, 0, AUX_SC1);
        }
      };
      Frags A, B;
      __builtin_amdgcn_s_setprio(1);
      BAR();
      STAMP(1);
      rd(A, 0, 0, 0, 3);
#pragma unroll 1
      for (unsigned j = 0; j < 4; ++j) {
        const unsigned slot = j * SLOT, slotn = ((j + 1) & 3) * SLOT;
        rd(B, slot, 0, 1, 3); mm(A, 3); il(6);
        if (j == 1) WAIT_V(0);
        BAR();
        rd(A, slot, 1, 0, 3); mm(B, 3); il(6);
        rd(B, slot, 1, 1, 3); mm(A, 3); il(6);
        BAR();
        rd(A, slotn, 0, 0, 3);
        mm(B, 3); il(6);
      }
      bf16x8 xk[16];
#pragma unroll
      for (int j = 0; j < 4; ++j) {
        const unsigned slot = j * SLOT;
        xk[4 * j] = A.x;
        rd(B, slot, 0, 1, 1); mm(A, 1); il(4);
        BAR();
        xk[4 * j + 1] = B.x;
        rd(A, slot, 1, 0, 1); mm(B, 1); il(4);
        xk[4 * j + 2] = A.x;
        rd(B, slot, 1, 1, 1); mm(A, 1); il(4);
        if (j == 3) asm volatile("s_waitcnt lgkmcnt(0)" ::: "memory");
        BAR();
        xk[4 * j + 3] = B.x;
        if (j < 3) { rd(A, slot + SLOT, 0, 0, 1); mm(B, 1); il(4); }
        else mm(B, 1);
      }
      STAMP(2);
      if (pf < 14) { epi(0); epi(1); }
      STAMP(8);
      BAR();
      STAMP(9);
      {
        auto rdb = [&](bf16x8 (&wb)[2], int t) {
#pragma unroll
          for (int a = 0; a < 2; ++a) {
            const unsigned ad = wr + (t >> 2) * SLOT + BOFF + ((t >> 1) & 1) * 4096 + a * 2048 + (t & 1) * 1024;
            wb[a] = trfrag(ad, ad + 256);
          }
        };
        bf16x8 wb[3][2];
        rdb(wb[0], 0); rdb(wb[1], 1);
#pragma unroll
        for (int t = 0; t < 16; ++t) {
          if (t + 2 < 16) rdb(wb[(t + 2) % 3], t + 2);
          acc[0][2] = MFMA32(wb[t % 3][0], xk[t], acc[0][2]);
          acc[1][2] = MFMA32(wb[t % 3][1], xk[t], acc[1][2]);
          SGB(0x008, 1); SGB(0x100, 2); SGB(0x008, 1); SGB(0x100, 2);
          __builtin_amdgcn_sched_barrier(0);
        }
      }
      STAMP(10);
      const unsigned fo = WS_FLAGS + ((4 * pm + mbw) * 32 + pf) * 128;
      WAIT_V(0);
      if (lane == 0 && pf < 14) __builtin_amdgcn_raw_buffer_store_b64(tg, rsWS, 0, fo, AUX_SC1);
      STAMP(11);
      epi(2);
      __builtin_amdgcn_s_setprio(0);
      STAMP(12);
      BAR();
      WAIT_V(0);
      if (lane == 0) __builtin_amdgcn_raw_buffer_store_b64(tg, rsWS, 0, fo + 2048, AUX_SC1);
      STAMP(3);
      LBAR();
    } else {
      BAR();
      if (w == 4 && lane == 0) __builtin_amdgcn_raw_buffer_store_b64(tg, rsWS, 0, WS_DONE + (32 + (xcd & 1) * 8 + pmi) * 128, AUX_SC1);
    }
    STAMP(4);
  }

  const unsigned hh = lane >> 5, blk = (lane >> 4) & 1, q2 = (lane & 15) >> 2, p2 = lane & 3;
  const unsigned dr0 = ldsb + kq * SLOT + (4 * hh + q2) * 128 + ((q2 >> 1) * 64) + blk * 32 + 8 * p2;
  const unsigned dr1 = dr0 ^ 64;
  f32x16 acc2[2];
#pragma unroll
  for (int i = 0; i < 16; ++i) { acc2[0][i] = 0.f; acc2[1][i] = 0.f; }
  asm volatile("" : "+v"(acc2[0]), "+v"(acc2[1]));
  {
    const unsigned mb2 = 2 * mt2 + mbl;
    const unsigned hb = WS_H + (mb2 * NKS) * 1024;
    const unsigned foff = WS_FLAGS + (mb2 * 32 + (lane & 31)) * 128;
    const unsigned loA = (7 * kq) >> 1, hiA = (7 * kq + 6) >> 1, loB = e2b >> 1, hiB = (e2b + nk - 15) >> 1;
    const unsigned needA = ((2u << hiA) - 1u) & ~((1u << loA) - 1u);
    const unsigned needB = (((2u << hiB) - 1u) & ~((1u << loB) - 1u)) << 16;
    unsigned seen = 0;
    auto wait_flags = [&](unsigned need) {
      for (unsigned spin = 0; (seen & need) != need && spin < (1u << 20); ++spin) {
        asm volatile("" ::: "memory");
        const bool want = lane < 32 && (((POLLALL ? (needA | needB) : need) & ~seen) >> lane & 1u);
        const v2u f = __builtin_amdgcn_raw_buffer_load_b64(rsWS, want ? foff : 0x40000000u, 0, AUX_SC1);
        seen |= (unsigned)__builtin_amdgcn_ballot_w64(want && f.x == tagx && f.y == tagy);
        if ((seen & need) != need) __builtin_amdgcn_s_sleep(PSLEEP);
      }
      __builtin_amdgcn_fence(__ATOMIC_ACQUIRE, "wavefront");
    };
    v4u hf[HPF];
    auto ldh = [&](int i) {
      if (i == 0) wait_flags(needA);
      if (i == 14) wait_flags(needB);
      const unsigned kap = i < 14 ? 7 * kq + (i < 7 ? i : i + 21) : i < 21 ? 56 + e2b + (i - 14) : 56 + e2b + nk - 15;
      return __builtin_amdgcn_raw_buffer_load_b128(rsWS, lane * 16, hb + kap * 1024, AUX_SC1);
    };
    bf16x8 dn0 = trfrag(dr0, dr0 + 1024), dn1 = trfrag(dr1, dr1 + 1024);
#pragma unroll
    for (int i = 0; i < HPF; ++i) { hf[i] = ldh(i); if (i == 0) { STAMP(5); } }
#pragma unroll
    for (int i = 0; i < 22; ++i) {
      __builtin_amdgcn_sched_barrier(0);
      if (i == 7) {
        LBAR();
        if (loader) { dmaDr(20, 21); if (nk == 22) dmaDr(21, 22); }
      }
      if (i == 7 + HPF + 1) LBAR();
      const bf16x8 d0 = dn0, d1 = dn1;
      if (i + 1 < 22) {
        const unsigned o = (i + 1 >= 20 ? i + 1 - 20 : i + 1) * 2048;
        dn0 = trfrag(dr0 + o, dr0 + o + 1024); dn1 = trfrag(dr1 + o, dr1 + o + 1024);
      }
      v4u hraw = hf[i % HPF];
      if (i == 21) { const unsigned msk = nk == 22 ? 0xffffffffu : 0u; hraw &= msk; }
      const bf16x8 hv = __builtin_bit_cast(bf16x8, hraw);
      acc2[0] = MFMA32(d0, hv, acc2[0]);
      acc2[1] = MFMA32(d1, hv, acc2[1]);
      if (i + HPF < 22) hf[i % HPF] = ldh(i + HPF);
      if (i == 7 + HPF + 1 && nt2 == 0 && w == 4 && lane == 0) {
        const v2u tg = {tagx, tagy};
        __builtin_amdgcn_raw_buffer_store_b64(tg, rsWS, 0, WS_DONE + mt2 * 128, AUX_SC1);
      }
    }
    __builtin_amdgcn_sched_barrier(0);
  }
  STAMP(6);
  v2u done0 = {0u, 0u};
  if (b == 0 && w == 4) done0 = __builtin_amdgcn_raw_buffer_load_b64(rsWS, lane * 128, WS_DONE, AUX_SC1);
  {
    const unsigned wa = ldsb + 4096 + (mbl * 4 + kq) * 2048 + lane * 16;
#pragma unroll
    for (int d = 0; d < 4; ++d)
#pragma unroll
      for (int g = 0; g < 2; ++g) {
        const int rb = 8 * (d & 1) + 4 * g;
        *(LAS f32x4*)(wa + d * SLOT + g * 1024) = f32x4{acc2[d >> 1][rb], acc2[d >> 1][rb + 1], acc2[d >> 1][rb + 2], acc2[d >> 1][rb + 3]};
      }
    const unsigned row = 64 * mt2 + 32 * mbl + (lane & 31);
    const unsigned col = 64 * nt2 + 16 * kq + 8 * hh;
    GAS v4u* op = (GAS v4u*)((GAS unsigned short*)po + row * D_ + col);
    LBAR();
    const unsigned ra = ldsb + kq * SLOT + 4096 + mbl * 8192 + lane * 16;
    f32x4 pv[4][2];
#pragma unroll
    for (int s = 0; s < 4; ++s)
#pragma unroll
      for (int g = 0; g < 2; ++g) pv[s][g] = *(LAS f32x4*)(ra + s * 2048 + g * 1024);
    const f32x4 s0 = (pv[0][0] + pv[1][0]) + (pv[2][0] + pv[3][0]), s1 = (pv[0][1] + pv[1][1]) + (pv[2][1] + pv[3][1]);
    const unsigned a0_ = cvtpk(f32x2{s0[0], s0[1]}), a1_ = cvtpk(f32x2{s0[2], s0[3]});
    const unsigned b0_ = cvtpk(f32x2{s1[0], s1[1]}), b1_ = cvtpk(f32x2{s1[2], s1[3]});
    auto t0 = __builtin_amdgcn_permlane32_swap(a0_, b0_, false, false);
    auto t1 = __builtin_amdgcn_permlane32_swap(a1_, b1_, false, false);
    *op = v4u{t0[0], t1[0], t0[1], t1[1]};
  }
  if (b == 0 && w == 4) {
    for (unsigned spin = 0; spin < (1u << 20); ++spin) {
      if (__all(lane >= 48 || (done0.x == tagx && done0.y == tagy))) break;
      asm volatile("" ::: "memory");
      done0 = __builtin_amdgcn_raw_buffer_load_b64(rsWS, lane * 128, WS_DONE, AUX_SC1);
    }
    if (lane == 0) { v2u tg = {tagx, tagy}; __builtin_amdgcn_raw_buffer_store_b64(tg, rsWS, 0, WS_EPOCH, AUX_SC1); }
  }
  STAMP(7);
  asm volatile(".Lffn_pf_end:");
}

extern "C" void kernel_launch(void* const* d_in, const int* in_sizes, int n_in, void* d_out, int out_size, void* d_ws, size_t ws_size,
                              hipStream_t stream) {
  (void)in_sizes; (void)n_in; (void)out_size; (void)ws_size;
  (void)hipFuncSetAttribute((const void*)ffn_fused, hipFuncAttributeMaxDynamicSharedMemorySize, 160 * 1024);
  ffn_fused<<<256, 512, 160 * 1024, stream>>>((const unsigned short*)d_in[0], (const unsigned short*)d_in[1], (const unsigned short*)d_in[2],
                                              (const unsigned short*)d_in[3], (unsigned short*)d_out, (unsigned char*)d_ws
                                              );
}
```

```cpp
#include <hip/hip_runtime.h>
#include <hip/hip_bf16.h>

typedef short bf16x8 __attribute__((ext_vector_type(8)));
typedef short s16x4 __attribute__((ext_vector_type(4)));
typedef float f32x4 __attribute__((ext_vector_type(4)));
typedef float f32x16 __attribute__((ext_vector_type(16)));
typedef float f32x2 __attribute__((ext_vector_type(2)));
typedef __bf16 bf16x2_t __attribute__((ext_vector_type(2)));
typedef unsigned v4u __attribute__((ext_vector_type(4)));
typedef unsigned v2u __attribute__((ext_vector_type(2)));
#define LAS __attribute__((address_space(3)))
#define GAS __attribute__((address_space(1)))
#define DI __device__ __forceinline__

constexpr int M_ = 2048, D_ = 512, F_ = 1376;
constexpr int WROW = F_ * 2;
constexpr int SLOT = 40960, XOFF = 16384, BOFF = 32768;
constexpr int NKS = F_ / 16;
constexpr int WS_EPOCH = 0;
constexpr int WS_DONE = 4096;
constexpr int WS_FLAGS = 16384;
constexpr int WS_H = 16384 + 262144;
constexpr int WS_BYTES = WS_H + 64 * NKS * 1024;
constexpr int AUX_SC1 = 16;
#define HPF 7
#define DK1 1
#define PSLEEP 4
#define POLLALL 0
#define WDPF 112
#define WDROT 1
#define CODEPF 1

DI unsigned rfl(unsigned x) { return __builtin_amdgcn_readfirstlane(x); }
DI void dma16(unsigned m0v, unsigned voff, __amdgpu_buffer_rsrc_t rs, unsigned soff) {
  asm volatile("s_mov_b32 m0, %0\n\ts_nop 0\n\tbuffer_load_dwordx4 %1, %2, %3 offen lds"
               :: "s"(m0v), "v"(voff), "s"(rs), "s"(soff) : "memory");
}
DI void pf4(unsigned m0v, unsigned voff, __amdgpu_buffer_rsrc_t rs) {
  asm volatile("s_mov_b32 m0, %0\n\ts_nop 0\n\tbuffer_load_dword %1, %2, 0 offen lds" :: "s"(m0v), "v"(voff), "s"(rs) : "memory");
}
DI unsigned cvtpk(f32x2 v) {
  bf16x2_t b = __builtin_convertvector(v, bf16x2_t);
  return __builtin_bit_cast(unsigned, b);
}
DI bf16x8 trfrag(unsigned addr0, unsigned addr1) {
  s16x4 a = __builtin_amdgcn_ds_read_tr16_b64_v4i16((LAS s16x4*)addr0);
  s16x4 b = __builtin_amdgcn_ds_read_tr16_b64_v4i16((LAS s16x4*)addr1);
  return __builtin_shufflevector(a, b, 0, 1, 2, 3, 4, 5, 6, 7);
}
DI unsigned silu_mul2(f32x2 g, f32x2 u) {
  const f32x2 t = g * -1.44269504088896340736f;
  f32x2 e = {__builtin_amdgcn_exp2f(t.x), __builtin_amdgcn_exp2f(t.y)};
  e = e + 1.0f;
  const f32x2 rc = {__builtin_amdgcn_rcpf(e.x), __builtin_amdgcn_rcpf(e.y)};
  return cvtpk((g * u) * rc);
}
#define WAIT_V(N) asm volatile("s_waitcnt vmcnt(" #N ")" ::: "memory")
#define BAR() asm volatile("s_barrier" ::: "memory")
#define LBAR() asm volatile("s_waitcnt lgkmcnt(0)\n\ts_barrier" ::: "memory")
#define VBAR(N) asm volatile("s_waitcnt vmcnt(" #N ")\n\ts_barrier" ::: "memory")
#define MFMA32(a, b, c) __builtin_amdgcn_mfma_f32_32x32x16_bf16((a), (b), (c), 0, 0, 0)
#define SGB(mask, n) __builtin_amdgcn_sched_group_barrier((mask), (n), 0)

#define STAMP(i)

struct Frags { bf16x8 x; bf16x8 wv[2][3]; };

__global__ __launch_bounds__(512, 2) void ffn_fused(const unsigned short* __restrict__ x, const unsigned short* __restrict__ wg,
                                                    const unsigned short* __restrict__ wu, const unsigned short* __restrict__ wd,
                                                    unsigned short* __restrict__ out, unsigned char* __restrict__ ws
                                                    ) {
  extern __shared__ __attribute__((aligned(16))) char lds_[];
  const unsigned ldsb = (unsigned)(size_t)lds_;
  const unsigned tid = threadIdx.x, lane = tid & 63;
  const unsigned w = rfl(tid >> 6);
  const unsigned b = blockIdx.x, xcd = b & 7, r = b >> 3;
  STAMP(0);

  const unsigned t1 = 30 * (xcd >> 1) + r;
  const unsigned pmi = t1 & 7, pf = t1 >> 3;
  const unsigned pm = 8 * (xcd & 1) + pmi;
  const bool producer = r < 30;
  const unsigned f0 = pf < 14 ? 96 * pf : 1280;
  const bool loader = w < 4;
  const unsigned kq = w & 3, mbl = w >> 2;

  unsigned long long px = (unsigned long long)x, pw = (unsigned long long)(kq < 2 ? wg : wu), pd = (unsigned long long)wd,
                     po = (unsigned long long)out, pws = (unsigned long long)ws;
  asm volatile("" : "+s"(px), "+s"(pw), "+s"(pd), "+s"(po), "+s"(pws));
  unsigned long long epoch = *(const __attribute__((address_space(4))) unsigned long long*)(pws + WS_EPOCH);

  if (loader && producer) {
    const unsigned lw = w;
    const __amdgpu_buffer_rsrc_t rsX = __builtin_amdgcn_make_buffer_rsrc((void*)px, (short)0, M_ * D_ * 2, 0x00020000);
    const __amdgpu_buffer_rsrc_t rsW = __builtin_amdgcn_make_buffer_rsrc((void*)pw, (short)0, D_ * F_ * 2, 0x00020000);
    const unsigned rot1 = (4 * (pmi & 1) + 2 * (xcd >> 1) + (xcd & 1)) & 7;
    const unsigned voffX0 = (lane >> 3) * 1024 + (((lane & 7) ^ (lane >> 4)) << 4);
    const unsigned voffX1 = voffX0 ^ 64;
    const unsigned voffW = (lane >> 2) * WROW + ((lane & 3) << 4);
    const unsigned sX = (pm * 128 + 16 * lw) * 1024;
    const unsigned sW = ((lw & 1) * 16) * WROW + f0 * 2;
    const unsigned mX = ldsb + XOFF + 16 * lw * 128;
    const unsigned mA = ldsb + (lw >> 1) * 4096 + (lw & 1) * 1024;
    const unsigned mB = ldsb + BOFF + (lw >> 1) * 2048 + (lw & 1) * 1024;
    auto dmaX = [&](unsigned xh) {
      const unsigned j = xh >> 1, h = xh & 1, sl = (j + rot1) & 7;
      const unsigned m0 = mX + (j & 3) * SLOT + h * 8192, so = sX + h * 65536 + sl * 128;
      dma16(m0, voffX0, rsX, so);
      dma16(m0 + 1024, voffX1, rsX, so + 8192);
    };
    auto dmaWA = [&](unsigned p) {
      const unsigned j = p >> 1, h = p & 1, sl = (j + rot1) & 7;
      const unsigned m0 = mA + (j & 3) * SLOT + h * 8192, so = sW + (sl * 64 + h * 32) * WROW;
      dma16(m0, voffW, rsW, so);
      dma16(m0 + 2048, voffW, rsW, so + 64);
    };
    auto dmaWB = [&](unsigned p) {
      const unsigned j = p >> 1, h = p & 1, sl = (j + rot1) & 7;
      dma16(mB + (j & 3) * SLOT + h * 4096, voffW, rsW, sW + (sl * 64 + h * 32) * WROW + 128);
    };
    dmaWA(0); dmaWB(0); dmaX(0); dmaX(1);
    dmaWA(1); dmaWB(1); dmaX(2);
    dmaWA(2); dmaWB(2); dmaX(3);
    asm volatile("" :: "s"(epoch));
    STAMP(1);
#pragma unroll 1
    for (unsigned p = 0; p < 5; ++p) {
      VBAR(10);
      dmaWA(p + 3); dmaWB(p + 3); dmaX(p + 4);
    }
#pragma unroll 1
    for (unsigned p = 5; p < 12; ++p) {
      VBAR(8);
      dmaWA(p + 3); dmaX(p + 4);
    }
    VBAR(8);
    dmaWA(15);
    VBAR(6);
    dmaWB(8); dmaWB(9); dmaWB(10); dmaWB(11);
    VBAR(6);
    dmaWB(12); dmaWB(13); dmaWB(14); dmaWB(15);
    VBAR(8);
    BAR();
    STAMP(2);
  }

  const __amdgpu_buffer_rsrc_t rsD = __builtin_amdgcn_make_buffer_rsrc((void*)pd, (short)0, F_ * D_ * 2, 0x00020000);
  const __amdgpu_buffer_rsrc_t rsWS = __builtin_amdgcn_make_buffer_rsrc((void*)pws, (short)0, WS_BYTES, 0x00020000);
  const unsigned mt2 = 4 * xcd + (r >> 3), nt2 = r & 7;
  const unsigned nk = kq < 2 ? 22 : 21;
  const unsigned e2b = kq * 8 - (kq >> 1 & kq & 1);
  const unsigned voffD = (lane >> 3) * 1024 + (((lane & 7) ^ (((lane >> 4) & 1) << 2)) << 4);
  auto dmaDr = [&](int i0, int i1) {
#pragma unroll
    for (int part = 0; part < 3; ++part) {
      const int lo = i0 > 7 * part ? i0 : 7 * part, hi = (part < 2 && i1 > 7 * part + 7) ? 7 * part + 7 : i1;
      if (lo < hi) {
        const unsigned e = (part < 2 ? 7 * kq : e2b) + (lo - 7 * part);
        unsigned m0 = ldsb + kq * SLOT + (lo >= 20 ? lo - 20 : lo) * 2048;
        unsigned so = (6 * (e >> 1) + (e & 1) + 2 * part) * 16384 + nt2 * 128;
        unsigned st = 16384 + (e & 1) * 65536;
#pragma unroll 1
        for (int i = lo; i < hi; ++i) {
          const unsigned sq = (part == 2 && so >= 88 * 16384) ? so - 65536 : so;
          dma16(m0, voffD, rsD, sq);
          dma16(m0 + 1024, voffD, rsD, sq + 8192);
          m0 += 2048; so += st; st ^= 65536;
        }
      }
    }
  };

  asm volatile("" : "+s"(epoch));
  const unsigned long long tag64 = epoch + 1ull;
  const unsigned tagx = (unsigned)tag64, tagy = (unsigned)(tag64 >> 32);

  if (!producer) {
    const unsigned junk = ldsb + 3 * SLOT + BOFF + w * 256;
    {
      const unsigned cc = xcd + 8 * ((r & 1) * 8 + w);
      unsigned row = 96 * (cc >> 2) + 64 + 8 * (cc & 3);
      row = row >= 1376 ? row - 64 : row;
      if (cc < 60) pf4(junk, row * 1024 + lane * 128, rsD);
    }
    unsigned c = (r & 1) * 8 + w + (WDROT ? xcd * (WDPF / 8) : 0);
#pragma unroll 1
    for (unsigned n = 0; n < WDPF / 16; ++n, c += 16) {
      c = c >= WDPF ? c - WDPF : c;
      const unsigned part = c >= 112 ? 2 : c >= 56 ? 1 : 0, cc = c - 56 * part;
      unsigned row = 96 * (cc >> 2) + 32 * part + 8 * (cc & 3);
      row = row >= 1376 ? row - 64 : row;
      pf4(junk, row * 1024 + lane * 128, rsD);
    }
    if (!loader) WAIT_V(0);
  }
  if (loader) {
    dmaDr(0, DK1);
    STAMP(8);
    if (producer) asm volatile("s_waitcnt vmcnt(%0)\n\ts_barrier" :: "n"(2 * DK1) : "memory");
    STAMP(9);
    dmaDr(DK1, 11);
    if (producer) BAR();
    dmaDr(11, 16);
    STAMP(10);
    VBAR(16);
    STAMP(3);
    dmaDr(16, 20);
    STAMP(4);
  } else {
    if (w == 4 && producer) {
      unsigned long long pc;
      unsigned len;
      asm volatile("s_getpc_b64 %0\n.Lffn_pf_here:\n\ts_mov_b32 %1, .Lffn_pf_end-.Lffn_pf_here" : "=s"(pc), "=s"(len));
      pf4(ldsb + 3 * SLOT, lane < 4 ? r * 512 + lane * 128 : 0x40000000u, __builtin_amdgcn_make_buffer_rsrc((void*)pc, (short)0, (int)len, 0x00020000));
    }
    const v2u tg = {tagx, tagy};
    if (producer) {
      const unsigned mbw = w & 3;
      const unsigned r32 = lane & 31, h2 = lane >> 5;
      unsigned xr[4];
#pragma unroll
      for (int c2 = 0; c2 < 4; ++c2) xr[c2] = ldsb + XOFF + (32 * mbw + r32) * 128 + (((2 * c2 + h2) ^ ((r32 >> 1) & 7)) << 4);
      const unsigned wr = ldsb + (8 * h2 + ((lane & 15) >> 2)) * 64 + ((lane >> 4) & 1) * 32 + 8 * (lane & 3);

      f32x16 acc[2][3];
#pragma unroll
      for (int a = 0; a < 2; ++a)
#pragma unroll
        for (int j = 0; j < 3; ++j) {
#pragma unroll
          for (int i = 0; i < 16; ++i) acc[a][j][i] = 0.f;
          asm volatile("" : "+v"(acc[a][j]));
        }
      auto rd = [&](Frags& Fr, unsigned slot, int h, int k, int mode) {
        Fr.x = *(LAS bf16x8*)(xr[2 * h + k] + slot);
#pragma unroll
        for (int a = 0; a < 2; ++a) {
          if (mode & 1) {
#pragma unroll
            for (int j = 0; j < 2; ++j) {
              const unsigned ad = wr + slot + h * 8192 + a * 4096 + j * 2048 + k * 1024;
              Fr.wv[a][j] = trfrag(ad, ad + 256);
            }
          }
          if (mode & 2) {
            const unsigned ad = wr + slot + BOFF + h * 4096 + a * 2048 + k * 1024;
            Fr.wv[a][2] = trfrag(ad, ad + 256);
          }
        }
      };
      auto mm = [&](const Frags& Fr, int mode) {
#pragma unroll
        for (int a = 0; a < 2; ++a)
#pragma unroll
          for (int j = 0; j < 3; ++j)
            if (j < 2 ? (mode & 1) : (mode & 2)) acc[a][j] = MFMA32(Fr.wv[a][j], Fr.x, acc[a][j]);
      };
      auto il = [&](int nm) {
        SGB(0x008, 1); SGB(0x100, 3);
#pragma unroll
        for (int i = 1; i < nm; ++i) { SGB(0x008, 1); SGB(0x100, 2); }
        __builtin_amdgcn_sched_barrier(0);
      };
      const unsigned hoff = lane * 16 + WS_H + ((4 * pm + mbw) * NKS + 2 * pf) * 1024;
      auto epi = [&](int j) {
#pragma unroll
        for (int sx = 0; sx < 2; ++sx) {
          v4u o;
#pragma unroll
          for (int e = 0; e < 4; ++e) {
            const int i0 = 8 * sx + 2 * e;
            o[e] = silu_mul2(f32x2{acc[0][j][i0], acc[0][j][i0 + 1]}, f32x2{acc[1][j][i0], acc[1][j][i0 + 1]});
          }
          __builtin_amdgcn_raw_buffer_store_b128(o, rsWS, hoff + (28 * j + sx) * 1024, 0, AUX_SC1);
        }
      };
      Frags A, B;
      __builtin_amdgcn_s_setprio(1);
      BAR();
      STAMP(1);
      rd(A, 0, 0, 0, 3);
#pragma unroll 1
      for (unsigned j = 0; j < 4; ++j) {
        const unsigned slot = j * SLOT, slotn = ((j + 1) & 3) * SLOT;
        rd(B, slot, 0, 1, 3); mm(A, 3); il(6);
        if (j == 1) WAIT_V(0);
        BAR();
        rd(A, slot, 1, 0, 3); mm(B, 3); il(6);
        rd(B, slot, 1, 1, 3); mm(A, 3); il(6);
        BAR();
        rd(A, slotn, 0, 0, 3);
        mm(B, 3); il(6);
      }
      bf16x8 xk[16];
#pragma unroll
      for (int j = 0; j < 4; ++j) {
        const unsigned slot = j * SLOT;
        xk[4 * j] = A.x;
        rd(B, slot, 0, 1, 1); mm(A, 1); il(4);
        BAR();
        xk[4 * j + 1] = B.x;
        rd(A, slot, 1, 0, 1); mm(B, 1); il(4);
        xk[4 * j + 2] = A.x;
        rd(B, slot, 1, 1, 1); mm(A, 1); il(4);
        if (j == 3) asm volatile("s_waitcnt lgkmcnt(0)" ::: "memory");
        BAR();
        xk[4 * j + 3] = B.x;
        if (j < 3) { rd(A, slot + SLOT, 0, 0, 1); mm(B, 1); il(4); }
        else mm(B, 1);
      }
      STAMP(2);
      if (pf < 14) { epi(0); epi(1); }
      STAMP(8);
      BAR();
      STAMP(9);
      {
        auto rdb = [&](bf16x8 (&wb)[2], int t) {
#pragma unroll
          for (int a = 0; a < 2; ++a) {
            const unsigned ad = wr + (t >> 2) * SLOT + BOFF + ((t >> 1) & 1) * 4096 + a * 2048 + (t & 1) * 1024;
            wb[a] = trfrag(ad, ad + 256);
          }
        };
        bf16x8 wb[3][2];
        rdb(wb[0], 0); rdb(wb[1], 1);
#pragma unroll
        for (int t = 0; t < 16; ++t) {
          if (t + 2 < 16) rdb(wb[(t + 2) % 3], t + 2);
          acc[0][2] = MFMA32(wb[t % 3][0], xk[t], acc[0][2]);
          acc[1][2] = MFMA32(wb[t % 3][1], xk[t], acc[1][2]);
          SGB(0x008, 1); SGB(0x100, 2); SGB(0x008, 1); SGB(0x100, 2);
          __builtin_amdgcn_sched_barrier(0);
        }
      }
      STAMP(10);
      const unsigned fo = WS_FLAGS + ((4 * pm + mbw) * 32 + pf) * 128;
      WAIT_V(0);
      if (lane == 0 && pf < 14) __builtin_amdgcn_raw_buffer_store_b64(tg, rsWS, 0, fo, AUX_SC1);
      STAMP(11);
      epi(2);
      STAMP(12);
      BAR();
      WAIT_V(0);
      if (lane == 0) __builtin_amdgcn_raw_buffer_store_b64(tg, rsWS, 0, fo + 2048, AUX_SC1);
      STAMP(3);
      LBAR();
    } else {
      BAR();
      if (w == 4 && lane == 0) __builtin_amdgcn_raw_buffer_store_b64(tg, rsWS, 0, WS_DONE + (32 + xcd * 2 + (r & 1)) * 128, AUX_SC1);
    }
    STAMP(4);
  }

  const unsigned hh = lane >> 5, blk = (lane >> 4) & 1, q2 = (lane & 15) >> 2, p2 = lane & 3;
  const unsigned dr0 = ldsb + kq * SLOT + (4 * hh + q2) * 128 + ((q2 >> 1) * 64) + blk * 32 + 8 * p2;
  const unsigned dr1 = dr0 ^ 64;
  f32x16 acc2[2];
#pragma unroll
  for (int i = 0; i < 16; ++i) { acc2[0][i] = 0.f; acc2[1][i] = 0.f; }
  asm volatile("" : "+v"(acc2[0]), "+v"(acc2[1]));
  {
    const unsigned mb2 = 2 * mt2 + mbl;
    const unsigned hb = WS_H + (mb2 * NKS) * 1024;
    const unsigned foff = WS_FLAGS + (mb2 * 32 + (lane & 31)) * 128;
    const unsigned loA = (7 * kq) >> 1, hiA = (7 * kq + 6) >> 1, loB = e2b >> 1, hiB = (e2b + nk - 15) >> 1;
    const unsigned needA = ((2u << hiA) - 1u) & ~((1u << loA) - 1u);
    const unsigned needB = (((2u << hiB) - 1u) & ~((1u << loB) - 1u)) << 16;
    unsigned seen = 0;
    auto wait_flags = [&](unsigned need) {
      for (unsigned spin = 0; (seen & need) != need && spin < (1u << 15); ++spin) {
        asm volatile("" ::: "memory");
        const bool want = lane < 32 && (((POLLALL ? (needA | needB) : need) & ~seen) >> lane & 1u);
        const v2u f = __builtin_amdgcn_raw_buffer_load_b64(rsWS, want ? foff : 0x40000000u, 0, AUX_SC1);
        seen |= (unsigned)__builtin_amdgcn_ballot_w64(want && f.x == tagx && f.y == tagy);
        if ((seen & need) != need) __builtin_amdgcn_s_sleep(PSLEEP);
      }
      __builtin_amdgcn_fence(__ATOMIC_ACQUIRE, "wavefront");
    };
    v4u hf[HPF];
    auto ldh = [&](int i) {
      if (i == 0) wait_flags(needA);
      if (i == 14) wait_flags(needB);
      const unsigned kap = i < 14 ? 7 * kq + (i < 7 ? i : i + 21) : i < 21 ? 56 + e2b + (i - 14) : 56 + e2b + nk - 15;
      return __builtin_amdgcn_raw_buffer_load_b128(rsWS, lane * 16, hb + kap * 1024, AUX_SC1);
    };
    bf16x8 dn0 = trfrag(dr0, dr0 + 1024), dn1 = trfrag(dr1, dr1 + 1024);
#pragma unroll
    for (int i = 0; i < HPF; ++i) { hf[i] = ldh(i); if (i == 0) { STAMP(5); } }
#pragma unroll
    for (int i = 0; i < 22; ++i) {
      __builtin_amdgcn_sched_barrier(0);
      if (i == 7) {
        LBAR();
        if (loader) { dmaDr(20, 21); if (nk == 22) dmaDr(21, 22); }
      }
      if (i == 7 + HPF + 1) LBAR();
      const bf16x8 d0 = dn0, d1 = dn1;
      if (i + 1 < 22) {
        const unsigned o = (i + 1 >= 20 ? i + 1 - 20 : i + 1) * 2048;
        dn0 = trfrag(dr0 + o, dr0 + o + 1024); dn1 = trfrag(dr1 + o, dr1 + o + 1024);
      }
      v4u hraw = hf[i % HPF];
      if (i == 21) { const unsigned msk = nk == 22 ? 0xffffffffu : 0u; hraw &= msk; }
      const bf16x8 hv = __builtin_bit_cast(bf16x8, hraw);
      acc2[0] = MFMA32(d0, hv, acc2[0]);
      acc2[1] = MFMA32(d1, hv, acc2[1]);
      if (i + HPF < 22) hf[i % HPF] = ldh(i + HPF);
      if (i == 7 + HPF + 1 && nt2 == 0 && w == 4 && lane == 0) {
        const v2u tg = {tagx, tagy};
        __builtin_amdgcn_raw_buffer_store_b64(tg, rsWS, 0, WS_DONE + mt2 * 128, AUX_SC1);
      }
    }
    __builtin_amdgcn_sched_barrier(0);
  }
  STAMP(6);
  v2u done0 = {0u, 0u};
  if (b == 0 && w == 4) done0 = __builtin_amdgcn_raw_buffer_load_b64(rsWS, lane * 128, WS_DONE, AUX_SC1);
  {
    const unsigned wa = ldsb + 4096 + (mbl * 4 + kq) * 2048 + lane * 16;
#pragma unroll
    for (int d = 0; d < 4; ++d)
#pragma unroll
      for (int g = 0; g < 2; ++g) {
        const int rb = 8 * (d & 1) + 4 * g;
        *(LAS f32x4*)(wa + d * SLOT + g * 1024) = f32x4{acc2[d >> 1][rb], acc2[d >> 1][rb + 1], acc2[d >> 1][rb + 2], acc2[d >> 1][rb + 3]};
      }
    const unsigned row = 64 * mt2 + 32 * mbl + (lane & 31);
    const unsigned col = 64 * nt2 + 16 * kq + 8 * hh;
    GAS v4u* op = (GAS v4u*)((GAS unsigned short*)po + row * D_ + col);
    LBAR();
    const unsigned ra = ldsb + kq * SLOT + 4096 + mbl * 8192 + lane * 16;
    f32x4 pv[4][2];
#pragma unroll
    for (int s = 0; s < 4; ++s)
#pragma unroll
      for (int g = 0; g < 2; ++g) pv[s][g] = *(LAS f32x4*)(ra + s * 2048 + g * 1024);
    const f32x4 s0 = (pv[0][0] + pv[1][0]) + (pv[2][0] + pv[3][0]), s1 = (pv[0][1] + pv[1][1]) + (pv[2][1] + pv[3][1]);
    const unsigned a0_ = cvtpk(f32x2{s0[0], s0[1]}), a1_ = cvtpk(f32x2{s0[2], s0[3]});
    const unsigned b0_ = cvtpk(f32x2{s1[0], s1[1]}), b1_ = cvtpk(f32x2{s1[2], s1[3]});
    auto t0 = __builtin_amdgcn_permlane32_swap(a0_, b0_, false, false);
    auto t1 = __builtin_amdgcn_permlane32_swap(a1_, b1_, false, false);
    *op = v4u{t0[0], t1[0], t0[1], t1[1]};
  }
  if (b == 0 && w == 4) {
    for (unsigned spin = 0; spin < (1u << 15); ++spin) {
      if (__all(lane >= 48 || (done0.x == tagx && done0.y == tagy))) break;
      asm volatile("" ::: "memory");
      done0 = __builtin_amdgcn_raw_buffer_load_b64(rsWS, lane * 128, WS_DONE, AUX_SC1);
    }
    if (lane == 0) { v2u tg = {tagx, tagy}; __builtin_amdgcn_raw_buffer_store_b64(tg, rsWS, 0, WS_EPOCH, AUX_SC1); }
  }
  STAMP(7);
  asm volatile(".Lffn_pf_end:");
}

extern "C" void kernel_launch(void* const* d_in, const int* in_sizes, int n_in, void* d_out, int out_size, void* d_ws, size_t ws_size,
                              hipStream_t stream) {
  (void)in_sizes; (void)n_in; (void)out_size; (void)ws_size;
  (void)hipFuncSetAttribute((const void*)ffn_fused, hipFuncAttributeMaxDynamicSharedMemorySize, 160 * 1024);
  ffn_fused<<<256, 512, 160 * 1024, stream>>>((const unsigned short*)d_in[0], (const unsigned short*)d_in[1], (const unsigned short*)d_in[2],
                                              (const unsigned short*)d_in[3], (unsigned short*)d_out, (unsigned char*)d_ws
                                              );
}
```

```cpp
#include <hip/hip_runtime.h>
#include <hip/hip_bf16.h>

typedef short bf16x8 __attribute__((ext_vector_type(8)));
typedef short s16x4 __attribute__((ext_vector_type(4)));
typedef float f32x4 __attribute__((ext_vector_type(4)));
typedef float f32x16 __attribute__((ext_vector_type(16)));
typedef float f32x2 __attribute__((ext_vector_type(2)));
typedef __bf16 bf16x2_t __attribute__((ext_vector_type(2)));
typedef unsigned v4u __attribute__((ext_vector_type(4)));
typedef unsigned v2u __attribute__((ext_vector_type(2)));
#define LAS __attribute__((address_space(3)))
#define GAS __attribute__((address_space(1)))
#define DI __device__ __forceinline__

constexpr int M_ = 2048, D_ = 512, F_ = 1376;
constexpr int WROW = F_ * 2;
constexpr int SLOT = 40960, XOFF = 16384, BOFF = 32768;
constexpr int NKS = F_ / 16;
constexpr int WS_EPOCH = 0;
constexpr int WS_DONE = 4096;
constexpr int WS_FLAGS = 16384;
constexpr int WS_H = 16384 + 262144;
constexpr int WS_BYTES = WS_H + 64 * NKS * 1024;
constexpr int AUX_SC1 = 16;
constexpr int HPF = 7;
constexpr int DK1 = 1;
constexpr int WDPF = 112;
constexpr int PSLEEP = 4;
constexpr unsigned OOB = 0x40000000u;

DI unsigned rfl(unsigned x) { return __builtin_amdgcn_readfirstlane(x); }
DI void dma16(unsigned m0v, unsigned voff, __amdgpu_buffer_rsrc_t rs, unsigned soff) {
  asm volatile("s_mov_b32 m0, %0\n\ts_nop 0\n\tbuffer_load_dwordx4 %1, %2, %3 offen lds"
               :: "s"(m0v), "v"(voff), "s"(rs), "s"(soff) : "memory");
}
DI void pf4(unsigned m0v, unsigned voff, __amdgpu_buffer_rsrc_t rs) {
  asm volatile("s_mov_b32 m0, %0\n\ts_nop 0\n\tbuffer_load_dword %1, %2, 0 offen lds" :: "s"(m0v), "v"(voff), "s"(rs) : "memory");
}
DI unsigned cvtpk(f32x2 v) {
  bf16x2_t b = __builtin_convertvector(v, bf16x2_t);
  return __builtin_bit_cast(unsigned, b);
}
DI bf16x8 trfrag(unsigned addr0, unsigned addr1) {
  s16x4 a = __builtin_amdgcn_ds_read_tr16_b64_v4i16((LAS s16x4*)addr0);
  s16x4 b = __builtin_amdgcn_ds_read_tr16_b64_v4i16((LAS s16x4*)addr1);
  return __builtin_shufflevector(a, b, 0, 1, 2, 3, 4, 5, 6, 7);
}
DI unsigned silu_mul2(f32x2 g, f32x2 u) {
  const f32x2 t = g * -1.44269504088896340736f;
  f32x2 e = {__builtin_amdgcn_exp2f(t.x), __builtin_amdgcn_exp2f(t.y)};
  e = e + 1.0f;
  const f32x2 rc = {__builtin_amdgcn_rcpf(e.x), __builtin_amdgcn_rcpf(e.y)};
  return cvtpk((g * rc) * u);
}
#define WAIT_V(N) asm volatile("s_waitcnt vmcnt(" #N ")" ::: "memory")
#define BAR() asm volatile("s_barrier" ::: "memory")
#define LBAR() asm volatile("s_waitcnt lgkmcnt(0)\n\ts_barrier" ::: "memory")
#define VBAR(N) asm volatile("s_waitcnt vmcnt(" #N ")\n\ts_barrier" ::: "memory")
#define MFMA32(a, b, c) __builtin_amdgcn_mfma_f32_32x32x16_bf16((a), (b), (c), 0, 0, 0)
#define SGB(mask, n) __builtin_amdgcn_sched_group_barrier((mask), (n), 0)


struct Frags { bf16x8 x; bf16x8 wv[2][3]; };

__global__ __launch_bounds__(512, 2) void ffn_fused(const unsigned short* __restrict__ x, const unsigned short* __restrict__ wg,
                                                    const unsigned short* __restrict__ wu, const unsigned short* __restrict__ wd,
                                                    unsigned short* __restrict__ out, unsigned char* __restrict__ ws) {
  extern __shared__ __attribute__((aligned(16))) char lds_[];
  const unsigned ldsb = (unsigned)(size_t)lds_;
  const unsigned tid = threadIdx.x, lane = tid & 63;
  const unsigned w = rfl(tid >> 6);
  const unsigned b = blockIdx.x, xcd = b & 7, r = b >> 3;

  const unsigned t1 = 30 * (xcd >> 1) + r;
  const unsigned pmi = t1 & 7, pf = t1 >> 3;
  const unsigned pm = 8 * (xcd & 1) + pmi;
  const bool producer = r < 30;
  const unsigned f0 = pf < 14 ? 96 * pf : 1280;
  const bool loader = w < 4;
  const unsigned kq = w & 3, mbl = w >> 2;

  unsigned long long px = (unsigned long long)x, pw = (unsigned long long)(kq < 2 ? wg : wu), pd = (unsigned long long)wd,
                     po = (unsigned long long)out, pws = (unsigned long long)ws;
  asm volatile("" : "+s"(px), "+s"(pw), "+s"(pd), "+s"(po), "+s"(pws));
  unsigned long long epoch = *(const __attribute__((address_space(4))) unsigned long long*)(pws + WS_EPOCH);

  if (loader && producer) {
    const unsigned lw = w;
    const __amdgpu_buffer_rsrc_t rsX = __builtin_amdgcn_make_buffer_rsrc((void*)px, (short)0, M_ * D_ * 2, 0x00020000);
    const __amdgpu_buffer_rsrc_t rsW = __builtin_amdgcn_make_buffer_rsrc((void*)pw, (short)0, D_ * F_ * 2, 0x00020000);
    const unsigned rot1 = (4 * (pmi & 1) + 2 * (xcd >> 1) + (xcd & 1)) & 7;
    const unsigned voffX0 = (lane >> 3) * 1024 + (((lane & 7) ^ (lane >> 4)) << 4);
    const unsigned voffX1 = voffX0 ^ 64;
    const unsigned voffW = (lane >> 2) * WROW + ((lane & 3) << 4);
    const unsigned sX = (pm * 128 + 16 * lw) * 1024;
    const unsigned sW = ((lw & 1) * 16) * WROW + f0 * 2;
    const unsigned mX = ldsb + XOFF + 16 * lw * 128;
    const unsigned mA = ldsb + (lw >> 1) * 4096 + (lw & 1) * 1024;
    const unsigned mB = ldsb + BOFF + (lw >> 1) * 2048 + (lw & 1) * 1024;
    auto dmaX = [&](unsigned xh) {
      const unsigned j = xh >> 1, h = xh & 1, sl = (j + rot1) & 7;
      const unsigned m0 = mX + (j & 3) * SLOT + h * 8192, so = sX + h * 65536 + sl * 128;
      dma16(m0, voffX0, rsX, so);
      dma16(m0 + 1024, voffX1, rsX, so + 8192);
    };
    auto dmaWA = [&](unsigned p) {
      const unsigned j = p >> 1, h = p & 1, sl = (j + rot1) & 7;
      const unsigned m0 = mA + (j & 3) * SLOT + h * 8192, so = sW + (sl * 64 + h * 32) * WROW;
      dma16(m0, voffW, rsW, so);
      dma16(m0 + 2048, voffW, rsW, so + 64);
    };
    auto dmaWB = [&](unsigned p) {
      const unsigned j = p >> 1, h = p & 1, sl = (j + rot1) & 7;
      dma16(mB + (j & 3) * SLOT + h * 4096, voffW, rsW, sW + (sl * 64 + h * 32) * WROW + 128);
    };
    dmaWA(0); dmaWB(0); dmaX(0); dmaX(1);
    dmaWA(1); dmaWB(1); dmaX(2);
    dmaWA(2); dmaWB(2); dmaX(3);
    asm volatile("" :: "s"(epoch));
#pragma unroll 1
    for (unsigned p = 0; p < 5; ++p) {
      VBAR(10);
      dmaWA(p + 3); dmaWB(p + 3); dmaX(p + 4);
    }
#pragma unroll 1
    for (unsigned p = 5; p < 12; ++p) {
      VBAR(8);
      dmaWA(p + 3); dmaX(p + 4);
    }
    VBAR(8);
    dmaWA(15);
    VBAR(6);
    dmaWB(8); dmaWB(9); dmaWB(10); dmaWB(11);
    VBAR(6);
    dmaWB(12); dmaWB(13); dmaWB(14); dmaWB(15);
    VBAR(8);
    BAR();
  }

  const __amdgpu_buffer_rsrc_t rsD = __builtin_amdgcn_make_buffer_rsrc((void*)pd, (short)0, F_ * D_ * 2, 0x00020000);
  const __amdgpu_buffer_rsrc_t rsWS = __builtin_amdgcn_make_buffer_rsrc((void*)pws, (short)0, WS_BYTES, 0x00020000);
  const unsigned mt2 = 4 * xcd + (r >> 3), nt2 = r & 7;
  const unsigned nk = kq < 2 ? 22 : 21;
  const unsigned e2b = kq * 8 - (kq >> 1 & kq & 1);
  const unsigned voffD = (lane >> 3) * 1024 + (((lane & 7) ^ (((lane >> 4) & 1) << 2)) << 4);
  auto dmaDr = [&](int i0, int i1) {
#pragma unroll
    for (int part = 0; part < 3; ++part) {
      const int lo = i0 > 7 * part ? i0 : 7 * part, hi = (part < 2 && i1 > 7 * part + 7) ? 7 * part + 7 : i1;
      if (lo < hi) {
        const unsigned e = (part < 2 ? 7 * kq : e2b) + (lo - 7 * part);
        unsigned m0 = ldsb + kq * SLOT + (lo >= 20 ? lo - 20 : lo) * 2048;
        unsigned so = (6 * (e >> 1) + (e & 1) + 2 * part) * 16384 + nt2 * 128;
        unsigned st = 16384 + (e & 1) * 65536;
#pragma unroll 1
        for (int i = lo; i < hi; ++i) {
          const unsigned sq = (part == 2 && so >= 88 * 16384) ? so - 65536 : so;
          dma16(m0, voffD, rsD, sq);
          dma16(m0 + 1024, voffD, rsD, sq + 8192);
          m0 += 2048; so += st; st ^= 65536;
        }
      }
    }
  };

  asm volatile("" : "+s"(epoch));
  const unsigned long long tag64 = epoch + 1ull;
  const unsigned tagx = (unsigned)tag64, tagy = (unsigned)(tag64 >> 32);

  if (!producer) {
    const unsigned junk = ldsb + 3 * SLOT + BOFF + w * 256;
    {
      const unsigned cc = xcd + 8 * ((r & 1) * 8 + w);
      unsigned row = 96 * (cc >> 2) + 64 + 8 * (cc & 3);
      row = row >= 1376 ? row - 64 : row;
      if (cc < 60) pf4(junk, row * 1024 + lane * 128, rsD);
    }
    unsigned c = (r & 1) * 8 + w + xcd * (WDPF / 8);
#pragma unroll 1
    for (unsigned n = 0; n < WDPF / 16; ++n, c += 16) {
      c = c >= WDPF ? c - WDPF : c;
      const unsigned part = c >= 112 ? 2 : c >= 56 ? 1 : 0, cc = c - 56 * part;
      unsigned row = 96 * (cc >> 2) + 32 * part + 8 * (cc & 3);
      row = row >= 1376 ? row - 64 : row;
      pf4(junk, row * 1024 + lane * 128, rsD);
    }
    if (!loader) WAIT_V(0);
  }
  if (loader) {
    dmaDr(0, DK1);
    if (producer) asm volatile("s_waitcnt vmcnt(%0)\n\ts_barrier" :: "n"(2 * DK1) : "memory");
    dmaDr(DK1, 9);
    if (producer) BAR();
    dmaDr(9, 16);
    VBAR(16);
    dmaDr(16, 20);
  } else {
    if (w == 4 && producer) {
      unsigned long long pc;
      unsigned len;
      asm volatile("s_getpc_b64 %0\n.Lffn_pf_here:\n\ts_mov_b32 %1, .Lffn_pf_end-.Lffn_pf_here" : "=s"(pc), "=s"(len));
      pf4(ldsb + 3 * SLOT, lane < 4 ? r * 512 + lane * 128 : OOB, __builtin_amdgcn_make_buffer_rsrc((void*)pc, (short)0, (int)len, 0x00020000));
    }
    const v2u tg = {tagx, tagy};
    if (producer) {
      const unsigned mbw = w & 3;
      const unsigned r32 = lane & 31, h2 = lane >> 5;
      unsigned xr[4];
#pragma unroll
      for (int c2 = 0; c2 < 4; ++c2) xr[c2] = ldsb + XOFF + (32 * mbw + r32) * 128 + (((2 * c2 + h2) ^ ((r32 >> 1) & 7)) << 4);
      const unsigned wr = ldsb + (8 * h2 + ((lane & 15) >> 2)) * 64 + ((lane >> 4) & 1) * 32 + 8 * (lane & 3);

      f32x16 acc[2][3];
#pragma unroll
      for (int a = 0; a < 2; ++a)
#pragma unroll
        for (int j = 0; j < 3; ++j) {
#pragma unroll
          for (int i = 0; i < 16; ++i) acc[a][j][i] = 0.f;
          asm volatile("" : "+v"(acc[a][j]));
        }
      auto rd = [&](Frags& Fr, unsigned slot, int h, int k, int mode) {
        Fr.x = *(LAS bf16x8*)(xr[2 * h + k] + slot);
#pragma unroll
        for (int a = 0; a < 2; ++a) {
          if (mode & 1) {
#pragma unroll
            for (int j = 0; j < 2; ++j) {
              const unsigned ad = wr + slot + h * 8192 + a * 4096 + j * 2048 + k * 1024;
              Fr.wv[a][j] = trfrag(ad, ad + 256);
            }
          }
          if (mode & 2) {
            const unsigned ad = wr + slot + BOFF + h * 4096 + a * 2048 + k * 1024;
            Fr.wv[a][2] = trfrag(ad, ad + 256);
          }
        }
      };
      auto mm = [&](const Frags& Fr, int mode) {
#pragma unroll
        for (int a = 0; a < 2; ++a)
#pragma unroll
          for (int j = 0; j < 3; ++j)
            if (j < 2 ? (mode & 1) : (mode & 2)) acc[a][j] = MFMA32(Fr.wv[a][j], Fr.x, acc[a][j]);
      };
      auto il = [&](int nm) {
        SGB(0x008, 1); SGB(0x100, 3);
#pragma unroll
        for (int i = 1; i < nm; ++i) { SGB(0x008, 1); SGB(0x100, 2); }
        __builtin_amdgcn_sched_barrier(0);
      };
      const unsigned hoff = lane * 16 + WS_H + ((4 * pm + mbw) * NKS + 2 * pf) * 1024;
      auto epi = [&](int j) {
#pragma unroll
        for (int sx = 0; sx < 2; ++sx) {
          v4u o;
#pragma unroll
          for (int e = 0; e < 4; ++e) {
            const int i0 = 8 * sx + 2 * e;
            o[e] = silu_mul2(f32x2{acc[0][j][i0], acc[0][j][i0 + 1]}, f32x2{acc[1][j][i0], acc[1][j][i0 + 1]});
          }
          __builtin_amdgcn_raw_buffer_store_b128(o, rsWS, hoff + (28 * j + sx) * 1024, 0, AUX_SC1);
        }
      };
      Frags A, B;
      __builtin_amdgcn_s_setprio(1);
      BAR();
      rd(A, 0, 0, 0, 3);
#pragma unroll 1
      for (unsigned j = 0; j < 4; ++j) {
        const unsigned slot = j * SLOT, slotn = ((j + 1) & 3) * SLOT;
        rd(B, slot, 0, 1, 3); mm(A, 3); il(6);
        if (j == 1) WAIT_V(0);
        BAR();
        rd(A, slot, 1, 0, 3); mm(B, 3); il(6);
        rd(B, slot, 1, 1, 3); mm(A, 3); il(6);
        BAR();
        rd(A, slotn, 0, 0, 3);
        mm(B, 3); il(6);
      }
      bf16x8 xk[16];
#pragma unroll
      for (int j = 0; j < 4; ++j) {
        const unsigned slot = j * SLOT;
        xk[4 * j] = A.x;
        rd(B, slot, 0, 1, 1); mm(A, 1); il(4);
        BAR();
        xk[4 * j + 1] = B.x;
        rd(A, slot, 1, 0, 1); mm(B, 1); il(4);
        xk[4 * j + 2] = A.x;
        rd(B, slot, 1, 1, 1); mm(A, 1); il(4);
        if (j == 3) asm volatile("s_waitcnt lgkmcnt(0)" ::: "memory");
        BAR();
        xk[4 * j + 3] = B.x;
        if (j < 3) { rd(A, slot + SLOT, 0, 0, 1); mm(B, 1); il(4); }
        else mm(B, 1);
      }
      if (pf < 14) { epi(0); epi(1); }
      BAR();
      {
        auto rdb = [&](bf16x8 (&wb)[2], int t) {
#pragma unroll
          for (int a = 0; a < 2; ++a) {
            const unsigned ad = wr + (t >> 2) * SLOT + BOFF + ((t >> 1) & 1) * 4096 + a * 2048 + (t & 1) * 1024;
            wb[a] = trfrag(ad, ad + 256);
          }
        };
        bf16x8 wb[3][2];
        rdb(wb[0], 0); rdb(wb[1], 1);
#pragma unroll
        for (int t = 0; t < 16; ++t) {
          if (t + 2 < 16) rdb(wb[(t + 2) % 3], t + 2);
          acc[0][2] = MFMA32(wb[t % 3][0], xk[t], acc[0][2]);
          acc[1][2] = MFMA32(wb[t % 3][1], xk[t], acc[1][2]);
          SGB(0x008, 1); SGB(0x100, 2); SGB(0x008, 1); SGB(0x100, 2);
          __builtin_amdgcn_sched_barrier(0);
        }
      }
      const unsigned fo = WS_FLAGS + ((4 * pm + mbw) * 32 + pf) * 128;
      WAIT_V(0);
      if (lane == 0 && pf < 14) __builtin_amdgcn_raw_buffer_store_b64(tg, rsWS, 0, fo, AUX_SC1);
      epi(2);
      BAR();
      WAIT_V(0);
      if (lane == 0) __builtin_amdgcn_raw_buffer_store_b64(tg, rsWS, 0, fo + 2048, AUX_SC1);
      LBAR();
    } else {
      BAR();
      if (w == 4 && lane == 0) __builtin_amdgcn_raw_buffer_store_b64(tg, rsWS, 0, WS_DONE + (32 + xcd * 2 + (r & 1)) * 128, AUX_SC1);
    }
  }

  const unsigned hh = lane >> 5, blk = (lane >> 4) & 1, q2 = (lane & 15) >> 2, p2 = lane & 3;
  const unsigned dr0 = ldsb + kq * SLOT + (4 * hh + q2) * 128 + ((q2 >> 1) * 64) + blk * 32 + 8 * p2;
  const unsigned dr1 = dr0 ^ 64;
  f32x16 acc2[2];
#pragma unroll
  for (int i = 0; i < 16; ++i) { acc2[0][i] = 0.f; acc2[1][i] = 0.f; }
  asm volatile("" : "+v"(acc2[0]), "+v"(acc2[1]));
  {
    const unsigned mb2 = 2 * mt2 + mbl;
    const unsigned hb = WS_H + (mb2 * NKS) * 1024;
    const unsigned foff = WS_FLAGS + (mb2 * 32 + (lane & 31)) * 128;
    const unsigned loA = (7 * kq) >> 1, hiA = (7 * kq + 6) >> 1, loB = e2b >> 1, hiB = (e2b + nk - 15) >> 1;
    const unsigned needA = ((2u << hiA) - 1u) & ~((1u << loA) - 1u);
    const unsigned needB = (((2u << hiB) - 1u) & ~((1u << loB) - 1u)) << 16;
    unsigned seen = 0;
    auto wait_flags = [&](unsigned need) {
      for (unsigned spin = 0; (seen & need) != need && spin < (1u << 15); ++spin) {
        asm volatile("" ::: "memory");
        const bool want = lane < 32 && ((need & ~seen) >> lane & 1u);
        const v2u f = __builtin_amdgcn_raw_buffer_load_b64(rsWS, want ? foff : OOB, 0, AUX_SC1);
        seen |= (unsigned)__builtin_amdgcn_ballot_w64(want && f.x == tagx && f.y == tagy);
        if ((seen & need) != need) __builtin_amdgcn_s_sleep(PSLEEP);
      }
      __builtin_amdgcn_fence(__ATOMIC_ACQUIRE, "wavefront");
    };
    v4u hf[HPF];
    auto ldh = [&](int i) {
      if (i == 0) wait_flags(needA);
      if (i == 14) wait_flags(needB);
      const unsigned kap = i < 14 ? 7 * kq + (i < 7 ? i : i + 21) : i < 21 ? 56 + e2b + (i - 14) : 56 + e2b + nk - 15;
      return __builtin_amdgcn_raw_buffer_load_b128(rsWS, lane * 16, hb + kap * 1024, AUX_SC1);
    };
    bf16x8 dn0 = trfrag(dr0, dr0 + 1024), dn1 = trfrag(dr1, dr1 + 1024);
#pragma unroll
    for (int i = 0; i < HPF; ++i) { hf[i] = ldh(i); }
#pragma unroll
    for (int i = 0; i < 22; ++i) {
      __builtin_amdgcn_sched_barrier(0);
      if (i == 7) {
        LBAR();
        if (loader) { dmaDr(20, 21); if (nk == 22) dmaDr(21, 22); }
      }
      if (i == 7 + HPF + 1) LBAR();
      bf16x8 d0 = dn0, d1 = dn1;
      if (i + 1 < 22) {
        const unsigned o = (i + 1 >= 20 ? i + 1 - 20 : i + 1) * 2048;
        dn0 = trfrag(dr0 + o, dr0 + o + 1024); dn1 = trfrag(dr1 + o, dr1 + o + 1024);
      }
      v4u hraw = hf[i % HPF];
      if (i == 21) {
        const unsigned msk = nk == 22 ? 0xffffffffu : 0u;
        hraw &= msk;
        d0 = __builtin_bit_cast(bf16x8, __builtin_bit_cast(v4u, d0) & msk);
        d1 = __builtin_bit_cast(bf16x8, __builtin_bit_cast(v4u, d1) & msk);
      }
      const bf16x8 hv = __builtin_bit_cast(bf16x8, hraw);
      acc2[0] = MFMA32(d0, hv, acc2[0]);
      acc2[1] = MFMA32(d1, hv, acc2[1]);
      if (i + HPF < 22) hf[i % HPF] = ldh(i + HPF);
      if (i == 7 + HPF + 1 && nt2 == 0 && w == 4 && lane == 0) {
        const v2u tg = {tagx, tagy};
        __builtin_amdgcn_raw_buffer_store_b64(tg, rsWS, 0, WS_DONE + mt2 * 128, AUX_SC1);
      }
    }
    __builtin_amdgcn_sched_barrier(0);
  }
  v2u done0 = {0u, 0u};
  if (b == 0 && w == 4) done0 = __builtin_amdgcn_raw_buffer_load_b64(rsWS, lane * 128, WS_DONE, AUX_SC1);
  {
    const unsigned wa = ldsb + 4096 + (mbl * 4 + kq) * 2048 + lane * 16;
#pragma unroll
    for (int d = 0; d < 4; ++d)
#pragma unroll
      for (int g = 0; g < 2; ++g) {
        const int rb = 8 * (d & 1) + 4 * g;
        *(LAS f32x4*)(wa + d * SLOT + g * 1024) = f32x4{acc2[d >> 1][rb], acc2[d >> 1][rb + 1], acc2[d >> 1][rb + 2], acc2[d >> 1][rb + 3]};
      }
    const unsigned row = 64 * mt2 + 32 * mbl + (lane & 31);
    const unsigned col = 64 * nt2 + 16 * kq + 8 * hh;
    GAS v4u* op = (GAS v4u*)((GAS unsigned short*)po + row * D_ + col);
    LBAR();
    const unsigned ra = ldsb + kq * SLOT + 4096 + mbl * 8192 + lane * 16;
    f32x4 pv[4][2];
#pragma unroll
    for (int s = 0; s < 4; ++s)
#pragma unroll
      for (int g = 0; g < 2; ++g) pv[s][g] = *(LAS f32x4*)(ra + s * 2048 + g * 1024);
    const f32x4 s0 = (pv[0][0] + pv[1][0]) + (pv[2][0] + pv[3][0]), s1 = (pv[0][1] + pv[1][1]) + (pv[2][1] + pv[3][1]);
    const unsigned a0_ = cvtpk(f32x2{s0[0], s0[1]}), a1_ = cvtpk(f32x2{s0[2], s0[3]});
    const unsigned b0_ = cvtpk(f32x2{s1[0], s1[1]}), b1_ = cvtpk(f32x2{s1[2], s1[3]});
    auto t0 = __builtin_amdgcn_permlane32_swap(a0_, b0_, false, false);
    auto t1 = __builtin_amdgcn_permlane32_swap(a1_, b1_, false, false);
    *op = v4u{t0[0], t1[0], t0[1], t1[1]};
  }
  if (b == 0 && w == 4) {
    for (unsigned spin = 0; spin < (1u << 15); ++spin) {
      if (__all(lane >= 48 || (done0.x == tagx && done0.y == tagy))) break;
      asm volatile("" ::: "memory");
      done0 = __builtin_amdgcn_raw_buffer_load_b64(rsWS, lane * 128, WS_DONE, AUX_SC1);
    }
    if (lane == 0) { v2u tg = {tagx, tagy}; __builtin_amdgcn_raw_buffer_store_b64(tg, rsWS, 0, WS_EPOCH, AUX_SC1); }
  }
  asm volatile(".Lffn_pf_end:");
}

extern "C" void kernel_launch(void* const* d_in, const int* in_sizes, int n_in, void* d_out, int out_size, void* d_ws, size_t ws_size,
                              hipStream_t stream) {
  (void)in_sizes; (void)n_in; (void)out_size; (void)ws_size;
  (void)hipFuncSetAttribute((const void*)ffn_fused, hipFuncAttributeMaxDynamicSharedMemorySize, 160 * 1024);
  ffn_fused<<<256, 512, 160 * 1024, stream>>>((const unsigned short*)d_in[0], (const unsigned short*)d_in[1], (const unsigned short*)d_in[2],
                                              (const unsigned short*)d_in[3], (unsigned short*)d_out, (unsigned char*)d_ws);
}
```
